```python
import torch

def _softmax(x, axis=-1):
    return torch.softmax(x, dim=axis)
def _gelu(x):
    return torch.nn.functional.gelu(x, approximate="tanh")
def _silu(x):
    return torch.nn.functional.silu(x)
def _sigmoid(x):
    return torch.sigmoid(x)
def _layernorm(x, axis=-1, eps=1e-5):
    m = x.mean(dim=axis, keepdim=True)
    v = x.var(dim=axis, keepdim=True, unbiased=False)
    return (x - m) / torch.sqrt(v + eps)
def _scatter_add(tgt, idx, src, axis=0):
    return tgt.clone().index_add_(axis, idx, src)
def _segment_sum(src, seg, n):
    out = torch.zeros((n,) + src.shape[1:], dtype=src.dtype)
    return out.index_add_(0, seg, src)
def _pad(x, pw):
    flat = tuple(v for pair in reversed(pw) for v in pair)
    return torch.nn.functional.pad(x, flat)

kernel_name = 'recurrent_scan__ssm_conv_s4d__dense_activation_squared_relu__prompt'

def setup_inputs():

    _w = (lambda a: (lambda t: t.to(torch.bfloat16) if t.is_floating_point() else t)(torch.as_tensor(a)))
    def _A_setup():
        Bsz, L, D, N = 1, 2048, 448, 256
        return {
            "u": _w((lambda *s: torch.randn(*s, dtype=torch.bfloat16) * 0.05)(Bsz, L, D)),
            "log_A_real": _w((lambda *s: torch.randn(*s, dtype=torch.bfloat16) * 3.2)(D, N) * 0.5),
            "A_imag": _w((lambda *s: torch.randn(*s, dtype=torch.bfloat16) * 0.05)(D, N) * 3.14),
            "C_real": _w((lambda *s: torch.randn(*s, dtype=torch.bfloat16) * 0.2)(D, N) / N ** 0.5),
            "C_imag": _w((lambda *s: torch.randn(*s, dtype=torch.bfloat16) * 0.2)(D, N) / N ** 0.5),
            "log_dt": _w((lambda *s: torch.randn(*s, dtype=torch.bfloat16) * 0.05)(D) * 0.5 - 4.0),
            "D_skip": _w((lambda *s: torch.randn(*s, dtype=torch.bfloat16) * 0.05)(D)),
        }
    _A_in = _A_setup()

    def _B_setup():
        B, S, D, F = 1, 2048, 448, 1792
        return {
            "x": _w((lambda *s: torch.randn(*s, dtype=torch.bfloat16) * 0.05)(B, S, D)),
            "w1": _w((lambda *s: torch.randn(*s, dtype=torch.bfloat16) * 0.05)(D, F)),
            "b1": _w((lambda *s: torch.randn(*s, dtype=torch.bfloat16) * 0.4)(F)),
            "w2": _w((lambda *s: torch.randn(*s, dtype=torch.bfloat16) * 0.05)(F, D)),
            "b2": _w((lambda *s: torch.randn(*s, dtype=torch.bfloat16) * 0.8)(D)),
        }
    _B_in = _B_setup()

    return {
        "A_u": _A_in["u"],
        "A_log_A_real": _A_in["log_A_real"],
        "A_A_imag": _A_in["A_imag"],
        "A_C_real": _A_in["C_real"],
        "A_C_imag": _A_in["C_imag"],
        "A_log_dt": _A_in["log_dt"],
        "A_D_skip": _A_in["D_skip"],
        "B_x": _B_in["x"],
        "B_w1": _B_in["w1"],
        "B_b1": _B_in["b1"],
        "B_w2": _B_in["w2"],
        "B_b2": _B_in["b2"],
    }

def reference(A_u, A_log_A_real, A_A_imag, A_C_real, A_C_imag, A_log_dt, A_D_skip, B_x, B_w1, B_b1, B_w2, B_b2):
    def _A_ref(u, log_A_real, A_imag, C_real, C_imag, log_dt, D_skip):
        L = u.shape[1]
        dt = torch.exp(log_dt)
        A = -torch.exp(log_A_real) + 1j * A_imag
        C = (C_real + 1j * C_imag) * dt[:, None]
        pos = (lambda x, dt: x.to(dt))(torch.arange(L), log_dt.dtype)
        P = torch.exp((A * dt[:, None])[:, :, None] * pos[None, None, :])
        K = 2.0 * torch.einsum("dn,dnl->dl", C, P).real
        L2 = 2 * L
        u_t = (lambda x, *d: x.permute(*d))(u, 0, 2, 1)
        Kf = (lambda x, *a, **k: torch.fft.rfft(x.float(), *a, **k))(K, L2)
        Uf = (lambda x, *a, **k: torch.fft.rfft(x.float(), *a, **k))(u_t, L2)
        y = (lambda x, *a, **k: torch.fft.irfft(x, *a, **k).to(torch.bfloat16))(Uf * Kf[None, :, :], L2)[:, :, :L]
        y = (lambda x, *d: x.permute(*d))(y, 0, 2, 1)
        return y + u * D_skip[None, None, :]

    def _B_ref(x, w1, b1, w2, b2):
        h = torch.einsum("bsd,df->bsf", x, w1) + b1
        zero = torch.zeros_like(h)
        r = torch.where(h > zero, h, zero)
        h = r * r
        out = torch.einsum("bsf,fd->bsd", h, w2) + b2
        return out

    x = A_u
    xn = _layernorm(x, axis=-1)
    s = _A_ref(xn, A_log_A_real, A_A_imag, A_C_real, A_C_imag, A_log_dt, A_D_skip)
    g = _silu(_B_ref(xn, B_w1, B_b1, B_w2, B_b2))
    return x + s * g
```

```cpp
#include <hip/hip_runtime.h>
#include <hip/hip_bf16.h>
#include <stdint.h>

typedef unsigned short u16;
typedef __attribute__((ext_vector_type(8))) short bf16x8;
typedef __attribute__((ext_vector_type(4))) short bf16x4;
typedef __attribute__((ext_vector_type(4))) float f32x4;
typedef __attribute__((ext_vector_type(16))) float f32x16;
typedef __attribute__((ext_vector_type(4))) unsigned int u32x4;
#define LDS_AS __attribute__((address_space(3)))

constexpr int SL = 2048;
constexpr int DM = 448;
constexpr int NMODE = 256;
constexpr int FF = 1792;
constexpr int KTAB = 48 * 16;

__device__ __forceinline__ float bf2f(u16 v) { return __uint_as_float(((unsigned)v) << 16); }
__device__ __forceinline__ u16 f2bf(float f) {
  __hip_bfloat16 b = __float2bfloat16(f);
  return *reinterpret_cast<u16*>(&b);
}
__device__ __forceinline__ float rbf(float f) { return bf2f(f2bf(f)); }

struct cf { float r, i; };
__device__ __forceinline__ cf cmul(cf a, cf b) {
  cf o; o.r = a.r * b.r - a.i * b.i; o.i = a.r * b.i + a.i * b.r; return o;
}
__device__ __forceinline__ cf cexp_p(float ar, float ai, float p) {
  float m = expf(ar * p);
  float s, c;
  sincosf(ai * p, &s, &c);
  cf o; o.r = m * c; o.i = m * s; return o;
}

constexpr int LN_ROWS = 32;
constexpr int LN_BLOCKS = SL / LN_ROWS;
constexpr int KG_RS = 136;
constexpr int K0_LDS = 4 * 4 * 16 * KG_RS * 2;

__device__ __forceinline__ void kg_store(u16* bh, u16* bl, int row, int lane, float re, float im) {
  u16 rh = f2bf(re), ih = f2bf(im);
  u16 rl = f2bf(re - bf2f(rh)), il = f2bf(im - bf2f(ih));
  *reinterpret_cast<unsigned*>(bh + row * KG_RS + 2 * lane) = (unsigned)rh | ((unsigned)ih << 16);
  *reinterpret_cast<unsigned*>(bl + row * KG_RS + 2 * lane) = (unsigned)rl | ((unsigned)il << 16);
}

__global__ __launch_bounds__(256) void k0_ln_kgen(
    const u16* __restrict__ u, const u16* __restrict__ logA, const u16* __restrict__ Aim,
    const u16* __restrict__ Cre, const u16* __restrict__ Cim, const u16* __restrict__ logdt,
    u16* __restrict__ xn, u16* __restrict__ xnT, float* __restrict__ ktab) {
  extern __shared__ __attribute__((aligned(16))) char smem[];
  const int tid = threadIdx.x, lane = tid & 63, w = tid >> 6;
  if (blockIdx.x < LN_BLOCKS) {
    u16* tile = reinterpret_cast<u16*>(smem);
    constexpr int TS = DM + 8;
    const int row0 = blockIdx.x * LN_ROWS + w * 8;
    u32x4 v[8];
    const bool act = lane < 56;
#pragma unroll
    for (int r = 0; r < 8; ++r) {
      v[r] = (u32x4){0u, 0u, 0u, 0u};
      if (act) v[r] = *reinterpret_cast<const u32x4*>(u + (size_t)(row0 + r) * DM + lane * 8);
    }
#pragma unroll
    for (int r = 0; r < 8; ++r) {
      float x[8];
#pragma unroll
      for (int j = 0; j < 4; ++j) {
        x[2 * j] = __uint_as_float(v[r][j] << 16);
        x[2 * j + 1] = __uint_as_float(v[r][j] & 0xffff0000u);
      }
      float s = 0.f;
#pragma unroll
      for (int j = 0; j < 8; ++j) s += x[j];
#pragma unroll
      for (int o = 32; o > 0; o >>= 1) s += __shfl_xor(s, o);
      const float mean = s * (1.0f / DM);
      float q = 0.f;
      if (act) {
#pragma unroll
        for (int j = 0; j < 8; ++j) { float dd = x[j] - mean; q += dd * dd; }
      }
#pragma unroll
      for (int o = 32; o > 0; o >>= 1) q += __shfl_xor(q, o);
      const float var_b = rbf(q * (1.0f / DM));
      const float mean_b = rbf(mean);
      const float den = rbf(sqrtf(rbf(var_b + 1e-5f)));
      u32x4 o4;
#pragma unroll
      for (int j = 0; j < 4; ++j) {
        u16 a = f2bf(rbf(x[2 * j] - mean_b) / den);
        u16 b = f2bf(rbf(x[2 * j + 1] - mean_b) / den);
        o4[j] = (unsigned)a | ((unsigned)b << 16);
      }
      if (act) {
        *reinterpret_cast<u32x4*>(xn + (size_t)(row0 + r) * DM + lane * 8) = o4;
        *reinterpret_cast<u32x4*>(tile + (w * 8 + r) * TS + lane * 8) = o4;
      }
    }
    __syncthreads();
    for (int d = tid; d < DM; d += 256) {
      unsigned pk[16];
#pragma unroll
      for (int r = 0; r < 16; ++r)
        pk[r] = (unsigned)tile[(2 * r) * TS + d] | ((unsigned)tile[(2 * r + 1) * TS + d] << 16);
      u32x4* dst = reinterpret_cast<u32x4*>(xnT + (size_t)d * SL + blockIdx.x * LN_ROWS);
#pragma unroll
      for (int c = 0; c < 4; ++c) dst[c] = (u32x4){pk[4 * c], pk[4 * c + 1], pk[4 * c + 2], pk[4 * c + 3]};
    }
    return;
  }
  const int d = blockIdx.x - LN_BLOCKS;
  u16* wbase = reinterpret_cast<u16*>(smem) + w * (4 * 16 * KG_RS);
  u16* Vh = wbase;
  u16* Vl = wbase + 16 * KG_RS;
  u16* Wh = wbase + 32 * KG_RS;
  u16* Wl = wbase + 48 * KG_RS;
  const int idx = d * NMODE + tid;
  const float dt = rbf(expf(bf2f(logdt[d])));
  const float Are = -rbf(expf(bf2f(logA[idx])));
  const float ar = Are * dt;
  const float ai = bf2f(Aim[idx]) * dt;
  cf C2; C2.r = 2.0f * (bf2f(Cre[idx]) * dt); C2.i = 2.0f * (bf2f(Cim[idx]) * dt);

  {
    cf V1 = cexp_p(ar, ai, 8.f);
    cf V8 = cexp_p(ar, ai, 64.f);
    cf V[8];
    V[0].r = 1.f; V[0].i = 0.f;
    V[1] = V1;
    V[2] = cmul(V1, V1);
    V[3] = cmul(V[2], V1);
    V[4] = cmul(V[2], V[2]);
    V[5] = cmul(V[4], V1);
    V[6] = cmul(V[4], V[2]);
    V[7] = cmul(V[4], V[3]);
#pragma unroll
    for (int b = 0; b < 8; ++b) kg_store(Vh, Vl, b, lane, V[b].r, V[b].i);
    kg_store(Vh, Vl, 8, lane, V8.r, V8.i);
#pragma unroll
    for (int b = 1; b < 8; ++b) { cf t = cmul(V8, V[b]); kg_store(Vh, Vl, 8 + b, lane, t.r, t.i); }
  }
  cf U[8];
  {
    cf E1 = cexp_p(ar, ai, 1.f);
    cf E2 = cmul(E1, E1);
    cf E3 = cmul(E2, E1);
    cf E4 = cmul(E2, E2);
    U[0] = C2;
    U[1] = cmul(C2, E1);
    U[2] = cmul(C2, E2);
    U[3] = cmul(C2, E3);
    U[4] = cmul(C2, E4);
    U[5] = cmul(U[4], E1);
    U[6] = cmul(U[4], E2);
    U[7] = cmul(U[4], E3);
  }
  const cf F1 = cexp_p(ar, ai, 128.f);
  const cf F2 = cexp_p(ar, ai, 256.f);
  const cf F4 = cexp_p(ar, ai, 512.f);
  const cf F8 = cexp_p(ar, ai, 1024.f);
  const cf F3 = cmul(F2, F1);

  f32x4 acc[3];
#pragma unroll
  for (int mt = 0; mt < 3; ++mt) acc[mt] = (f32x4){0.f, 0.f, 0.f, 0.f};
  const int fr = lane & 15, fq = lane >> 4;

  auto do_mfma = [&](int mt) {
    __syncthreads();
#pragma unroll
    for (int s = 0; s < 4; ++s) {
      const int off = fr * KG_RS + 32 * s + 8 * fq;
      bf16x8 ah = *reinterpret_cast<const bf16x8*>(Wh + off);
      bf16x8 al = *reinterpret_cast<const bf16x8*>(Wl + off);
      bf16x8 bh = *reinterpret_cast<const bf16x8*>(Vh + off);
      bf16x8 bl = *reinterpret_cast<const bf16x8*>(Vl + off);
      acc[mt] = __builtin_amdgcn_mfma_f32_16x16x32_bf16(ah, bh, acc[mt], 0, 0, 0);
      acc[mt] = __builtin_amdgcn_mfma_f32_16x16x32_bf16(ah, bl, acc[mt], 0, 0, 0);
      acc[mt] = __builtin_amdgcn_mfma_f32_16x16x32_bf16(al, bh, acc[mt], 0, 0, 0);
    }
    __syncthreads();
  };
#pragma unroll
  for (int e = 0; e < 8; ++e) kg_store(Wh, Wl, e, lane, U[e].r, -U[e].i);
#pragma unroll
  for (int e = 0; e < 8; ++e) { cf t = cmul(F1, U[e]); kg_store(Wh, Wl, 8 + e, lane, t.r, -t.i); }
  do_mfma(0);
#pragma unroll
  for (int e = 0; e < 4; ++e) {
    cf t = cmul(F2, U[2 * e]); kg_store(Wh, Wl, e, lane, t.r, -t.i);
    cf t2 = cmul(F3, U[2 * e]); kg_store(Wh, Wl, 4 + e, lane, t2.r, -t2.i);
  }
  {
    cf G[4];
    G[0] = F4; G[1] = cmul(F4, F1); G[2] = cmul(F4, F2); G[3] = cmul(F4, F3);
#pragma unroll
    for (int a = 0; a < 4; ++a) {
      cf t = cmul(G[a], U[0]); kg_store(Wh, Wl, 8 + 2 * a, lane, t.r, -t.i);
      cf t2 = cmul(G[a], U[4]); kg_store(Wh, Wl, 8 + 2 * a + 1, lane, t2.r, -t2.i);
    }
    do_mfma(1);
    cf H0 = cmul(F8, U[0]);
    kg_store(Wh, Wl, 0, lane, H0.r, -H0.i);
    { cf t = cmul(H0, F1); kg_store(Wh, Wl, 1, lane, t.r, -t.i); }
    { cf t = cmul(H0, F2); kg_store(Wh, Wl, 2, lane, t.r, -t.i); }
    { cf t = cmul(H0, F3); kg_store(Wh, Wl, 3, lane, t.r, -t.i); }
#pragma unroll
    for (int a = 0; a < 4; ++a) { cf t = cmul(H0, G[a]); kg_store(Wh, Wl, 4 + a, lane, t.r, -t.i); }
    { cf t = cmul(H0, F8); kg_store(Wh, Wl, 8, lane, t.r, -t.i); }
#pragma unroll
    for (int r = 9; r < 16; ++r) kg_store(Wh, Wl, r, lane, 0.f, 0.f);
    do_mfma(2);
  }
  float* red = reinterpret_cast<float*>(smem);
#pragma unroll
  for (int mt = 0; mt < 3; ++mt)
#pragma unroll
    for (int j = 0; j < 4; ++j) red[w * KTAB + (mt * 16 + fq * 4 + j) * 16 + fr] = acc[mt][j];
  __syncthreads();
  for (int i = tid; i < KTAB; i += 256)
    ktab[(size_t)d * KTAB + i] = (red[i] + red[KTAB + i]) + (red[2 * KTAB + i] + red[3 * KTAB + i]);
}

constexpr int G1_BM = 128, G1_BN = 112, G1_BK = 64;
constexpr int G1_BLOCKS = (SL / G1_BM) * (FF / G1_BN);
constexpr int G1_ABYTES = G1_BM * G1_BK * 2;
constexpr int G1_BRS = 224;
constexpr int G1_BBYTES = G1_BK * G1_BRS;
constexpr int G1_STAGE = G1_ABYTES + G1_BBYTES;
constexpr int CV_XS = 4160;
constexpr int CV_XBYTES = 8 * CV_XS;
constexpr int CV_KB = 144;
constexpr int CV_KPAD = 31;
constexpr int CV_KBYTES = (CV_KPAD + 64) * CV_KB;
constexpr int CV_TBYTES = KTAB * 4;
constexpr int K1_LDS = 2 * G1_STAGE;

__device__ __forceinline__ int ktab_index(int l) {
  int pos, r, b;
  if (l < 256) { pos = l; r = ((pos >> 7) << 3) + (pos & 7); b = (pos >> 3) & 15; return r * 16 + b; }
  pos = (int)bf2f(f2bf((float)l));
  if (pos < 512) { int q = pos - 256; r = 16 + ((q >> 7) << 2) + ((q >> 1) & 3); b = (q >> 3) & 15; }
  else if (pos < 1024) { int q = pos - 512; r = 24 + ((q >> 7) << 1) + ((q >> 2) & 1); b = (q >> 3) & 15; }
  else if (pos < 2048) { int q = pos - 1024; r = 32 + (q >> 7); b = (q >> 3) & 15; }
  else { r = 40; b = 0; }
  return r * 16 + b;
}

__global__ __launch_bounds__(256, 2) void k1_gemm1_conv(
    const u16* __restrict__ xn, const u16* __restrict__ xnT, const float* __restrict__ ktab,
    const u16* __restrict__ w1, const u16* __restrict__ b1, const u16* __restrict__ dskip,
    u16* __restrict__ hbuf, u16* __restrict__ sT) {
  extern __shared__ __attribute__((aligned(16))) char smem[];
  const int tid = threadIdx.x, lane = tid & 63, w = tid >> 6;
  if (blockIdx.x < G1_BLOCKS) {
    const int bid = blockIdx.x;
    const int xcd = bid & 7, rest = bid >> 3;
    const int mt_i = xcd * 2 + (rest & 1);
    const int nt_i = rest >> 1;
    const int m0 = mt_i * G1_BM, n0 = nt_i * G1_BN;
    const int fr = lane & 15, fq = lane >> 4;
    u32x4 ra[4], rb[4];
    auto gload = [&](int kt) {
#pragma unroll
      for (int i = 0; i < 4; ++i) {
        int c = tid + i * 256;
        int row = c >> 3, ch = c & 7;
        ra[i] = *reinterpret_cast<const u32x4*>(xn + (size_t)(m0 + row) * DM + kt * G1_BK + ch * 8);
      }
#pragma unroll
      for (int i = 0; i < 4; ++i) {
        int c = tid + i * 256;
        if (c < 896) {
          int kr = c / 14, ch = c - kr * 14;
          rb[i] = *reinterpret_cast<const u32x4*>(w1 + (size_t)(kt * G1_BK + kr) * FF + n0 + ch * 8);
        }
      }
    };
    auto swrite = [&](int buf) {
      char* A = smem + buf * G1_STAGE;
      char* B = A + G1_ABYTES;
#pragma unroll
      for (int i = 0; i < 4; ++i) {
        int c = tid + i * 256;
        int row = c >> 3, ch = c & 7;
        *reinterpret_cast<u32x4*>(A + row * 128 + ((ch ^ (row & 7)) << 4)) = ra[i];
      }
#pragma unroll
      for (int i = 0; i < 4; ++i) {
        int c = tid + i * 256;
        if (c < 896) {
          int kr = c / 14, ch = c - kr * 14;
          *reinterpret_cast<u32x4*>(B + kr * G1_BRS + ch * 16) = rb[i];
        }
      }
    };
    f32x4 acc[2][7];
#pragma unroll
    for (int i = 0; i < 2; ++i)
#pragma unroll
      for (int j = 0; j < 7; ++j) acc[i][j] = (f32x4){0.f, 0.f, 0.f, 0.f};
    constexpr int NKT = DM / G1_BK;
    gload(0);
    swrite(0);
    __syncthreads();
    for (int kt = 0; kt < NKT; ++kt) {
      const int buf = kt & 1;
      if (kt + 1 < NKT) gload(kt + 1);
      const char* A = smem + buf * G1_STAGE;
      const char* B = A + G1_ABYTES;
#pragma unroll
      for (int ks = 0; ks < 2; ++ks) {
        bf16x8 af[2];
#pragma unroll
        for (int i = 0; i < 2; ++i) {
          int row = w * 32 + i * 16 + fr;
          int ch = ks * 4 + fq;
          af[i] = *reinterpret_cast<const bf16x8*>(A + row * 128 + ((ch ^ (row & 7)) << 4));
        }
#pragma unroll
        for (int j = 0; j < 7; ++j) {
          const int q = (lane & 15) >> 2, p = lane & 3;
          const char* b0 = B + (ks * 32 + fq * 8 + q) * G1_BRS + j * 32 + p * 8;
          bf16x4 lo4 = __builtin_amdgcn_ds_read_tr16_b64_v4i16((LDS_AS bf16x4*)(b0));
          bf16x4 hi4 = __builtin_amdgcn_ds_read_tr16_b64_v4i16((LDS_AS bf16x4*)(b0 + 4 * G1_BRS));
          bf16x8 bfr = __builtin_shufflevector(lo4, hi4, 0, 1, 2, 3, 4, 5, 6, 7);
#pragma unroll
          for (int i = 0; i < 2; ++i)
            acc[i][j] = __builtin_amdgcn_mfma_f32_16x16x32_bf16(bfr, af[i], acc[i][j], 0, 0, 0);
        }
      }
      if (kt + 1 < NKT) swrite(buf ^ 1);
      __syncthreads();
    }
    u16* T = reinterpret_cast<u16*>(smem);
    constexpr int TS = G1_BN + 8;
#pragma unroll
    for (int j = 0; j < 7; ++j) {
      float bb[4];
#pragma unroll
      for (int r = 0; r < 4; ++r) bb[r] = bf2f(b1[n0 + j * 16 + fq * 4 + r]);
#pragma unroll
      for (int i = 0; i < 2; ++i) {
        u16 o[4];
#pragma unroll
        for (int r = 0; r < 4; ++r) {
          float hv = rbf(rbf(acc[i][j][r]) + bb[r]);
          hv = hv > 0.f ? hv : 0.f;
          o[r] = f2bf(hv * hv);
        }
        uint2 pk;
        pk.x = (unsigned)o[0] | ((unsigned)o[1] << 16);
        pk.y = (unsigned)o[2] | ((unsigned)o[3] << 16);
        *reinterpret_cast<uint2*>(T + (w * 32 + i * 16 + fr) * TS + j * 16 + fq * 4) = pk;
      }
    }
    __syncthreads();
    for (int c = tid; c < G1_BM * 14; c += 256) {
      int row = c / 14, ch = c - row * 14;
      u32x4 v = *reinterpret_cast<const u32x4*>(T + row * TS + ch * 8);
      *reinterpret_cast<u32x4*>(hbuf + (size_t)(m0 + row) * FF + n0 + ch * 8) = v;
    }
    return;
  }
  const int d = blockIdx.x - G1_BLOCKS;
  char* XS = smem;
  char* KM = smem + CV_XBYTES;
  float* KT = reinterpret_cast<float*>(smem + CV_XBYTES + CV_KBYTES);
  for (int i = tid; i < KTAB; i += 256) KT[i] = ktab[(size_t)d * KTAB + i];
  {
    const u16* xrow = xnT + (size_t)d * SL;
    for (int g = tid; g <= 259; g += 256) {
      int gg = g - 3;
      int q = 256 - gg;
      u32x4 cur = (u32x4){0u, 0u, 0u, 0u}, prv = (u32x4){0u, 0u, 0u, 0u};
      if (gg >= 0 && gg < 256) cur = *reinterpret_cast<const u32x4*>(xrow + gg * 8);
      if (gg >= 1 && gg <= 256) prv = *reinterpret_cast<const u32x4*>(xrow + (gg - 1) * 8);
      unsigned Dw[8] = {prv[0], prv[1], prv[2], prv[3], cur[0], cur[1], cur[2], cur[3]};
#pragma unroll
      for (int c = 0; c < 8; ++c) {
        u32x4 o;
#pragma unroll
        for (int k = 0; k < 4; ++k) {
          const int la = 8 + c - 2 * k;
          unsigned val;
          if (la & 1) {
            unsigned dd = Dw[(la - 1) >> 1];
            val = (dd >> 16) | (dd << 16);
          } else {
            val = (Dw[la >> 1] & 0xffffu) | (Dw[(la >> 1) - 1] & 0xffff0000u);
          }
          o[k] = val;
        }
        *reinterpret_cast<u32x4*>(XS + c * CV_XS + q * 16) = o;
      }
    }
  }
  for (int i = tid; i < CV_KPAD * CV_KB / 16; i += 256)
    *reinterpret_cast<u32x4*>(KM + i * 16) = (u32x4){0u, 0u, 0u, 0u};
  __syncthreads();
  {
    const int l0 = tid * 8;
    unsigned hi[4], lo[4];
#pragma unroll
    for (int j = 0; j < 4; ++j) {
      float v0 = KT[ktab_index(l0 + 2 * j)], v1 = KT[ktab_index(l0 + 2 * j + 1)];
      u16 h0 = f2bf(v0), h1 = f2bf(v1);
      u16 q0 = f2bf(v0 - bf2f(h0)), q1 = f2bf(v1 - bf2f(h1));
      hi[j] = (unsigned)h0 | ((unsigned)h1 << 16);
      lo[j] = (unsigned)q0 | ((unsigned)q1 << 16);
    }
    char* dst = KM + (CV_KPAD + (l0 >> 5)) * CV_KB + (l0 & 31) * 2;
    *reinterpret_cast<u32x4*>(dst) = (u32x4){hi[0], hi[1], hi[2], hi[3]};
    *reinterpret_cast<u32x4*>(dst + 64) = (u32x4){lo[0], lo[1], lo[2], lo[3]};
  }
  __syncthreads();
  f32x16 acc0, acc1;
#pragma unroll
  for (int i = 0; i < 16; ++i) { acc0[i] = 0.f; acc1[i] = 0.f; }
  {
    const int t0 = lane & 31, hh = lane >> 5;
    const int c = t0 & 7;
    const char* abase = XS + c * CV_XS + 2 * (2048 - 8 * (t0 >> 3) + 8 * hh);
    const char* bbase = KM + (CV_KPAD + t0) * CV_KB + 16 * hh;
#pragma unroll 2
    for (int it = 0; it < 8; ++it) {
      const int dl = w + 4 * it;
#pragma unroll
      for (int ks = 0; ks < 2; ++ks) {
        bf16x8 a0 = *reinterpret_cast<const bf16x8*>(abase - 64 * dl + 32 * ks);
        bf16x8 a1 = *reinterpret_cast<const bf16x8*>(abase - 64 * (dl + 32) + 32 * ks);
        const char* b0p = bbase - dl * CV_KB + 32 * ks;
        bf16x8 b0h = *reinterpret_cast<const bf16x8*>(b0p);
        bf16x8 b0l = *reinterpret_cast<const bf16x8*>(b0p + 64);
        bf16x8 b1h = *reinterpret_cast<const bf16x8*>(b0p + 32 * CV_KB);
        bf16x8 b1l = *reinterpret_cast<const bf16x8*>(b0p + 32 * CV_KB + 64);
        acc0 = __builtin_amdgcn_mfma_f32_32x32x16_bf16(a0, b0h, acc0, 0, 0, 0);
        acc1 = __builtin_amdgcn_mfma_f32_32x32x16_bf16(a0, b1h, acc1, 0, 0, 0);
        acc0 = __builtin_amdgcn_mfma_f32_32x32x16_bf16(a0, b0l, acc0, 0, 0, 0);
        acc1 = __builtin_amdgcn_mfma_f32_32x32x16_bf16(a0, b1l, acc1, 0, 0, 0);
        acc1 = __builtin_amdgcn_mfma_f32_32x32x16_bf16(a1, b0h, acc1, 0, 0, 0);
        acc1 = __builtin_amdgcn_mfma_f32_32x32x16_bf16(a1, b0l, acc1, 0, 0, 0);
      }
    }
  }
  __syncthreads();
  {
    float* red = reinterpret_cast<float*>(smem);
    const int col = lane & 31, hh = lane >> 5;
#pragma unroll
    for (int g = 0; g < 4; ++g) {
      f32x4 v0 = (f32x4){acc0[4 * g], acc0[4 * g + 1], acc0[4 * g + 2], acc0[4 * g + 3]};
      f32x4 v1 = (f32x4){acc1[4 * g], acc1[4 * g + 1], acc1[4 * g + 2], acc1[4 * g + 3]};
      *reinterpret_cast<f32x4*>(red + w * 2048 + 32 * col + 8 * g + 4 * hh) = v0;
      *reinterpret_cast<f32x4*>(red + w * 2048 + 1024 + 32 * col + 8 * g + 4 * hh) = v1;
    }
    __syncthreads();
    const float dsk = bf2f(dskip[d]);
    const int t = tid * 8;
    u32x4 xv = *reinterpret_cast<const u32x4*>(xnT + (size_t)d * SL + t);
    u32x4 o;
#pragma unroll
    for (int j = 0; j < 4; ++j) {
      u16 r2[2];
#pragma unroll
      for (int e = 0; e < 2; ++e) {
        int tt = t + 2 * j + e;
        float y = (red[tt] + red[2048 + tt]) + (red[4096 + tt] + red[6144 + tt]);
        float xnv = e ? __uint_as_float(xv[j] & 0xffff0000u) : __uint_as_float(xv[j] << 16);
        r2[e] = f2bf(rbf(y) + rbf(xnv * dsk));
      }
      o[j] = (unsigned)r2[0] | ((unsigned)r2[1] << 16);
    }
    *reinterpret_cast<u32x4*>(sT + (size_t)d * SL + t) = o;
  }
}

constexpr int G2_BM = 64, G2_BN = 64, G2_BK = 64;
constexpr int G2_BLOCKS = (SL / G2_BM) * (DM / G2_BN);
constexpr int G2_ABYTES = G2_BM * G2_BK * 2;
constexpr int G2_BBYTES = G2_BK * G2_BN * 2;
constexpr int G2_STAGE = G2_ABYTES + G2_BBYTES;
constexpr int K2_LDS = 2 * G2_STAGE;

__global__ __launch_bounds__(256, 2) void k2_gemm2(
    const u16* __restrict__ hbuf, const u16* __restrict__ w2, const u16* __restrict__ b2,
    const u16* __restrict__ u, const u16* __restrict__ sT, u16* __restrict__ out) {
  extern __shared__ __attribute__((aligned(16))) char smem[];
  const int tid = threadIdx.x, lane = tid & 63, w = tid >> 6;
  const int bid = blockIdx.x;
  const int xcd = bid & 7, rest = bid >> 3;
  const int mt_i = xcd * 4 + (rest & 3);
  const int nt_i = rest >> 2;
  const int m0 = mt_i * G2_BM, n0 = nt_i * G2_BN;
  const int fr = lane & 15, fq = lane >> 4;
  const int wm = w & 1, wn = w >> 1;
  u32x4 ra[2], rb[2];
  auto gload = [&](int kt) {
#pragma unroll
    for (int i = 0; i < 2; ++i) {
      int c = tid + i * 256; int row = c >> 3, ch = c & 7;
      ra[i] = *reinterpret_cast<const u32x4*>(hbuf + (size_t)(m0 + row) * FF + kt * G2_BK + ch * 8);
      rb[i] = *reinterpret_cast<const u32x4*>(w2 + (size_t)(kt * G2_BK + row) * DM + n0 + ch * 8);
    }
  };
  auto bswz = [](int r) { return (((r >> 1) & 1) << 1) | (((r >> 3) & 1) << 2); };
  auto swrite = [&](int buf) {
    char* A = smem + buf * G2_STAGE;
    char* B = A + G2_ABYTES;
#pragma unroll
    for (int i = 0; i < 2; ++i) {
      int c = tid + i * 256; int row = c >> 3, ch = c & 7;
      *reinterpret_cast<u32x4*>(A + row * 128 + ((ch ^ (row & 7)) << 4)) = ra[i];
      *reinterpret_cast<u32x4*>(B + row * 128 + ((ch ^ bswz(row)) << 4)) = rb[i];
    }
  };
  f32x4 acc[2][2];
#pragma unroll
  for (int i = 0; i < 2; ++i)
#pragma unroll
    for (int j = 0; j < 2; ++j) acc[i][j] = (f32x4){0.f, 0.f, 0.f, 0.f};
  constexpr int NKT = FF / G2_BK;
  gload(0);
  swrite(0);
  __syncthreads();
  for (int kt = 0; kt < NKT; ++kt) {
    const int buf = kt & 1;
    if (kt + 1 < NKT) gload(kt + 1);
    const char* A = smem + buf * G2_STAGE;
    const char* B = A + G2_ABYTES;
#pragma unroll
    for (int ks = 0; ks < 2; ++ks) {
      bf16x8 af[2];
#pragma unroll
      for (int i = 0; i < 2; ++i) {
        int row = wm * 32 + i * 16 + fr;
        int ch = ks * 4 + fq;
        af[i] = *reinterpret_cast<const bf16x8*>(A + row * 128 + ((ch ^ (row & 7)) << 4));
      }
#pragma unroll
      for (int j = 0; j < 2; ++j) {
        const int q = (lane & 15) >> 2, p = lane & 3;
        const int cb = wn * 2 + j;
        const int r0 = ks * 32 + fq * 8 + q, r1 = r0 + 4;
        const int ch = 2 * cb + (p >> 1);
        const char* p0 = B + r0 * 128 + ((ch ^ bswz(r0)) << 4) + (p & 1) * 8;
        const char* p1 = B + r1 * 128 + ((ch ^ bswz(r1)) << 4) + (p & 1) * 8;
        bf16x4 lo4 = __builtin_amdgcn_ds_read_tr16_b64_v4i16((LDS_AS bf16x4*)(p0));
        bf16x4 hi4 = __builtin_amdgcn_ds_read_tr16_b64_v4i16((LDS_AS bf16x4*)(p1));
        bf16x8 bfr = __builtin_shufflevector(lo4, hi4, 0, 1, 2, 3, 4, 5, 6, 7);
#pragma unroll
        for (int i = 0; i < 2; ++i)
          acc[i][j] = __builtin_amdgcn_mfma_f32_16x16x32_bf16(bfr, af[i], acc[i][j], 0, 0, 0);
      }
    }
    if (kt + 1 < NKT) swrite(buf ^ 1);
    __syncthreads();
  }
  u16* G = reinterpret_cast<u16*>(smem);
  u16* S = G + 64 * 72;
#pragma unroll
  for (int i = 0; i < 2; ++i) {
    int c = tid + i * 256; int nr = c >> 3, ch = c & 7;
    *reinterpret_cast<u32x4*>(S + nr * 72 + ch * 8) =
        *reinterpret_cast<const u32x4*>(sT + (size_t)(n0 + nr) * SL + m0 + ch * 8);
  }
#pragma unroll
  for (int j = 0; j < 2; ++j) {
    const int nb = wn * 32 + j * 16 + fq * 4;
    float bb[4];
#pragma unroll
    for (int r = 0; r < 4; ++r) bb[r] = bf2f(b2[n0 + nb + r]);
#pragma unroll
    for (int i = 0; i < 2; ++i) {
      u16 o[4];
#pragma unroll
      for (int r = 0; r < 4; ++r) {
        float z = rbf(rbf(acc[i][j][r]) + bb[r]);
        float g = z / (1.0f + expf(-z));
        o[r] = f2bf(g);
      }
      uint2 pk;
      pk.x = (unsigned)o[0] | ((unsigned)o[1] << 16);
      pk.y = (unsigned)o[2] | ((unsigned)o[3] << 16);
      *reinterpret_cast<uint2*>(G + (wm * 32 + i * 16 + fr) * 72 + nb) = pk;
    }
  }
  __syncthreads();
#pragma unroll
  for (int i = 0; i < 2; ++i) {
    int c = tid + i * 256; int row = c >> 3, ch = c & 7;
    u32x4 gv = *reinterpret_cast<const u32x4*>(G + row * 72 + ch * 8);
    u32x4 uv = *reinterpret_cast<const u32x4*>(u + (size_t)(m0 + row) * DM + n0 + ch * 8);
    u32x4 o;
#pragma unroll
    for (int j = 0; j < 4; ++j) {
      u16 r2[2];
#pragma unroll
      for (int e = 0; e < 2; ++e) {
        float g = e ? __uint_as_float(gv[j] & 0xffff0000u) : __uint_as_float(gv[j] << 16);
        float xv = e ? __uint_as_float(uv[j] & 0xffff0000u) : __uint_as_float(uv[j] << 16);
        float s = bf2f(S[(ch * 8 + 2 * j + e) * 72 + row]);
        r2[e] = f2bf(xv + rbf(s * g));
      }
      o[j] = (unsigned)r2[0] | ((unsigned)r2[1] << 16);
    }
    *reinterpret_cast<u32x4*>(out + (size_t)(m0 + row) * DM + n0 + ch * 8) = o;
  }
}

extern "C" void kernel_launch(void* const* d_in, const int* in_sizes, int n_in,
                              void* d_out, int out_size, void* d_ws, size_t ws_size,
                              hipStream_t stream) {
  const u16* u = (const u16*)d_in[0];
  const u16* logA = (const u16*)d_in[1];
  const u16* Aim = (const u16*)d_in[2];
  const u16* Cre = (const u16*)d_in[3];
  const u16* Cim = (const u16*)d_in[4];
  const u16* logdt = (const u16*)d_in[5];
  const u16* dskip = (const u16*)d_in[6];
  const u16* w1 = (const u16*)d_in[8];
  const u16* b1 = (const u16*)d_in[9];
  const u16* w2 = (const u16*)d_in[10];
  const u16* b2 = (const u16*)d_in[11];
  u16* out = (u16*)d_out;
  char* ws = (char*)d_ws;
  u16* xn = (u16*)(ws);
  u16* xnT = (u16*)(ws + (2u << 20));
  u16* sT = (u16*)(ws + (4u << 20));
  float* ktab = (float*)(ws + (6u << 20));
  u16* hbuf = (u16*)(ws + (8u << 20));

  hipFuncSetAttribute((const void*)k0_ln_kgen, hipFuncAttributeMaxDynamicSharedMemorySize, K0_LDS);
  hipFuncSetAttribute((const void*)k1_gemm1_conv, hipFuncAttributeMaxDynamicSharedMemorySize, K1_LDS);
  hipFuncSetAttribute((const void*)k2_gemm2, hipFuncAttributeMaxDynamicSharedMemorySize, K2_LDS);
  k0_ln_kgen<<<LN_BLOCKS + DM, 256, K0_LDS, stream>>>(u, logA, Aim, Cre, Cim, logdt, xn, xnT, ktab);
  k1_gemm1_conv<<<G1_BLOCKS + DM, 256, K1_LDS, stream>>>(xn, xnT, ktab, w1, b1, dskip, hbuf, sT);
  k2_gemm2<<<G2_BLOCKS, 256, K2_LDS, stream>>>(hbuf, w2, b2, u, sT, out);
}
```

```cpp
#include <hip/hip_runtime.h>
#include <hip/hip_bf16.h>
#include <stdint.h>
#include <type_traits>

#if defined(__HIP_DEVICE_COMPILE__)
#pragma clang attribute push(__attribute__((target("no-packed-fp32-ops"))), apply_to = function)
#endif
typedef unsigned short u16;
typedef __attribute__((ext_vector_type(8))) short bf16x8;
typedef __attribute__((ext_vector_type(4))) short bf16x4;
typedef __attribute__((ext_vector_type(8))) _Float16 f16x8;
typedef __attribute__((ext_vector_type(2))) _Float16 f16x2;
typedef __attribute__((ext_vector_type(2))) __bf16 bf16x2;
typedef __attribute__((ext_vector_type(2))) float f32x2;
typedef __attribute__((ext_vector_type(4))) float f32x4;
typedef __attribute__((ext_vector_type(16))) float f32x16;
typedef __attribute__((ext_vector_type(4))) unsigned int u32x4;
typedef __attribute__((ext_vector_type(2))) unsigned int u32x2;
#define LDS_AS __attribute__((address_space(3)))

#define TIDX ((int)__builtin_amdgcn_workitem_id_x())
#define BIDX ((int)__builtin_amdgcn_workgroup_id_x())
__device__ __forceinline__ float u2f(unsigned v) { return __builtin_bit_cast(float, v); }
__device__ __forceinline__ unsigned f2u(float v) { return __builtin_bit_cast(unsigned, v); }
__device__ __forceinline__ void bsync() {
  __builtin_amdgcn_fence(__ATOMIC_RELEASE, "workgroup");
  __builtin_amdgcn_s_barrier();
  __builtin_amdgcn_fence(__ATOMIC_ACQUIRE, "workgroup");
}
__device__ __forceinline__ float shfl_xor_f(float v, int m, int lane) {
  return u2f((unsigned)__builtin_amdgcn_ds_bpermute((lane ^ m) << 2, (int)f2u(v)));
}
__device__ __forceinline__ float exp_acc(float x) {
  const float L2E_HI = 1.44269502162933349609375f, L2E_LO = 1.92596299112661746e-8f;
  float t = x * L2E_HI;
  float e = __builtin_fmaf(x, L2E_HI, -t) + x * L2E_LO;
  float r = __builtin_amdgcn_exp2f(t);
  return __builtin_fmaf(r, e * 0.693147180559945309f, r);
}

constexpr int SL = 2048;
constexpr int DM = 448;
constexpr int NMODE = 256;
constexpr int FF = 1792;
constexpr int KTAB = 48 * 16;

__device__ __forceinline__ float bf2f(u16 v) { return u2f(((unsigned)v) << 16); }
__device__ __forceinline__ unsigned pk2bf(float a, float b) {
  f32x2 v = {a, b};
  bf16x2 r = __builtin_convertvector(v, bf16x2);
  return *reinterpret_cast<unsigned*>(&r);
}
__device__ __forceinline__ u16 f2bf(float f) { return (u16)(pk2bf(f, 0.f) & 0xffffu); }
__device__ __forceinline__ float rbf(float f) { return u2f(pk2bf(f, 0.f) << 16); }
__device__ __forceinline__ float lo_f(unsigned v) { return u2f(v << 16); }
__device__ __forceinline__ float hi_f(unsigned v) { return u2f(v & 0xffff0000u); }
__device__ __forceinline__ unsigned pk2h(float a, float b) {
  f32x2 v = {a, b};
  f16x2 r = __builtin_convertvector(v, f16x2);
  return *reinterpret_cast<unsigned*>(&r);
}

template <int I, int N> struct Unroll {
  template <class F> static __device__ __forceinline__ void run(F& f) {
    f(std::integral_constant<int, I>{});
    Unroll<I + 1, N>::run(f);
  }
};
template <int N> struct Unroll<N, N> {
  template <class F> static __device__ __forceinline__ void run(F&) {}
};
template <int N> __device__ __forceinline__ void wait_vm() {
  asm volatile("s_waitcnt vmcnt(%0)" ::"n"(N) : "memory");
}
__device__ __forceinline__ void wait_lgkm0() { asm volatile("s_waitcnt lgkmcnt(0)" ::: "memory"); }

template <int OFF> __device__ __forceinline__ bf16x4 tr_read(unsigned addr) {
  bf16x4 r;
  asm volatile("ds_read_b64_tr_b16 %0, %1 offset:%2" : "=v"(r) : "v"(addr), "n"(OFF));
  return r;
}
__device__ __forceinline__ unsigned lds_addr(const void* p) { return (unsigned)(size_t)p; }

#define STAMPK(k, s)
struct cf { float r, i; };
__device__ __forceinline__ cf cmul(cf a, cf b) {
  cf o; o.r = a.r * b.r - a.i * b.i; o.i = a.r * b.i + a.i * b.r; return o;
}
__device__ __forceinline__ cf cexp_p(float ar, float ai, float p) {
  float m = __builtin_amdgcn_exp2f((ar * p) * 1.4426950408889634f);
  float rev = (ai * p) * 0.15915494309189535f;
  rev = __builtin_amdgcn_fractf(rev);
  cf o; o.r = m * __builtin_amdgcn_cosf(rev); o.i = m * __builtin_amdgcn_sinf(rev); return o;
}
template <int CTRL> __device__ __forceinline__ float dpp_add(float s) {
  int t = __builtin_amdgcn_update_dpp(0, (int)f2u(s), CTRL, 0xf, 0xf, true);
  return s + u2f((unsigned)t);
}
__device__ __forceinline__ float red16(float s) {
  s = dpp_add<0xB1>(s);
  s = dpp_add<0x4E>(s);
  s = dpp_add<0x141>(s);
  s = dpp_add<0x140>(s);
  return s;
}

constexpr int LN_ROWS = 16;
constexpr int LN_BLOCKS = SL / LN_ROWS;
constexpr int KG_RS = 136;
constexpr int K0_LDS = 4 * 4 * 16 * KG_RS * 2;

__device__ __forceinline__ int ktab_index(int l) {
  int pos, r, b;
  if (l < 256) { pos = l; r = ((pos >> 7) << 3) + (pos & 7); b = (pos >> 3) & 15; return r * 16 + b; }
  pos = (int)rbf((float)l);
  if (pos < 512) { int q = pos - 256; r = 16 + ((q >> 7) << 2) + ((q >> 1) & 3); b = (q >> 3) & 15; }
  else if (pos < 1024) { int q = pos - 512; r = 24 + ((q >> 7) << 1) + ((q >> 2) & 1); b = (q >> 3) & 15; }
  else if (pos < 2048) { int q = pos - 1024; r = 32 + (q >> 7); b = (q >> 3) & 15; }
  else { r = 40; b = 0; }
  return r * 16 + b;
}

__device__ __forceinline__ void kg_store(u16* bh, u16* bl, int row, int lane, float re, float im) {
  unsigned h = pk2bf(re, im);
  unsigned l = pk2bf(re - lo_f(h), im - hi_f(h));
  *reinterpret_cast<unsigned*>(bh + row * KG_RS + 2 * lane) = h;
  *reinterpret_cast<unsigned*>(bl + row * KG_RS + 2 * lane) = l;
}

#define NOPK_ATTR
__global__ __launch_bounds__(256) NOPK_ATTR void k0_ln_kgen(
    const u16* __restrict__ u, const u16* __restrict__ logA, const u16* __restrict__ Aim,
    const u16* __restrict__ Cre, const u16* __restrict__ Cim, const u16* __restrict__ logdt,
    u16* __restrict__ xn, u16* __restrict__ xnT, u16* __restrict__ kexp, float* __restrict__ kinvp) {
  extern __shared__ __attribute__((aligned(16))) char smem[];
  const int tid = TIDX, lane = tid & 63, w = tid >> 6;
  if (BIDX < LN_BLOCKS) {
    u16* tile = reinterpret_cast<u16*>(smem);
    constexpr int TS = DM + 8;
    STAMPK(0, 0);
    const int rb = tid >> 4, l16 = tid & 15;
    const int row = BIDX * LN_ROWS + rb;
    u32x2 v[7];
#pragma unroll
    for (int i = 0; i < 7; ++i)
      v[i] = *reinterpret_cast<const u32x2*>(u + (size_t)row * DM + i * 64 + l16 * 4);
    float x[28];
#pragma unroll
    for (int i = 0; i < 7; ++i) {
      x[4 * i] = lo_f(v[i][0]); x[4 * i + 1] = hi_f(v[i][0]);
      x[4 * i + 2] = lo_f(v[i][1]); x[4 * i + 3] = hi_f(v[i][1]);
    }
    float s = 0.f;
#pragma unroll
    for (int j = 0; j < 28; ++j) s += x[j];
    s = red16(s);
    const float mean = s * (1.0f / DM);
    float q = 0.f;
#pragma unroll
    for (int j = 0; j < 28; ++j) { float dd = x[j] - mean; q += dd * dd; }
    q = red16(q);
    const float var_b = rbf(q * (1.0f / DM));
    const float mean_b = rbf(mean);
    const float den = rbf(__builtin_sqrtf(rbf(var_b + 1e-5f)));
#pragma unroll
    for (int i = 0; i < 7; ++i) {
      u32x2 o;
      o[0] = pk2bf(rbf(x[4 * i] - mean_b) / den, rbf(x[4 * i + 1] - mean_b) / den);
      o[1] = pk2bf(rbf(x[4 * i + 2] - mean_b) / den, rbf(x[4 * i + 3] - mean_b) / den);
      *reinterpret_cast<u32x2*>(xn + (size_t)row * DM + i * 64 + l16 * 4) = o;
      *reinterpret_cast<u32x2*>(tile + rb * TS + i * 64 + l16 * 4) = o;
    }
    bsync();
    STAMPK(0, 1);
    for (int d = tid; d < DM; d += 256) {
      unsigned pk[8];
#pragma unroll
      for (int r = 0; r < 8; ++r)
        pk[r] = (unsigned)tile[(2 * r) * TS + d] | ((unsigned)tile[(2 * r + 1) * TS + d] << 16);
      u32x4* dst = reinterpret_cast<u32x4*>(xnT + (size_t)d * SL + BIDX * LN_ROWS);
      dst[0] = (u32x4){pk[0], pk[1], pk[2], pk[3]};
      dst[1] = (u32x4){pk[4], pk[5], pk[6], pk[7]};
    }
    STAMPK(0, 5);
    return;
  }
  const int d = BIDX - LN_BLOCKS;
  u16* wbase = reinterpret_cast<u16*>(smem) + w * (4 * 16 * KG_RS);
  u16* Vh = wbase;
  u16* Vl = wbase + 16 * KG_RS;
  u16* Wh = wbase + 32 * KG_RS;
  u16* Wl = wbase + 48 * KG_RS;
  STAMPK(0, 0);
  const int idx = d * NMODE + tid;
  const u16 r_ldt = logdt[d], r_la = logA[idx], r_ai = Aim[idx], r_cr = Cre[idx], r_ci = Cim[idx];
  const float dt = rbf(exp_acc(bf2f(r_ldt)));
  const float Are = -rbf(exp_acc(bf2f(r_la)));
  const float ar = Are * dt;
  const float ai = bf2f(r_ai) * dt;
  cf C2; C2.r = 2.0f * (bf2f(r_cr) * dt); C2.i = 2.0f * (bf2f(r_ci) * dt);

  {
    cf V1 = cexp_p(ar, ai, 8.f);
    cf V8 = cexp_p(ar, ai, 64.f);
    cf V[8];
    V[0].r = 1.f; V[0].i = 0.f;
    V[1] = V1;
    V[2] = cmul(V1, V1);
    V[3] = cmul(V[2], V1);
    V[4] = cmul(V[2], V[2]);
    V[5] = cmul(V[4], V1);
    V[6] = cmul(V[4], V[2]);
    V[7] = cmul(V[4], V[3]);
#pragma unroll
    for (int b = 0; b < 8; ++b) kg_store(Vh, Vl, b, lane, V[b].r, V[b].i);
    kg_store(Vh, Vl, 8, lane, V8.r, V8.i);
#pragma unroll
    for (int b = 1; b < 8; ++b) { cf t = cmul(V8, V[b]); kg_store(Vh, Vl, 8 + b, lane, t.r, t.i); }
  }
  cf U[8];
  {
    cf E1 = cexp_p(ar, ai, 1.f);
    cf E2 = cmul(E1, E1);
    cf E3 = cmul(E2, E1);
    cf E4 = cmul(E2, E2);
    U[0] = C2;
    U[1] = cmul(C2, E1);
    U[2] = cmul(C2, E2);
    U[3] = cmul(C2, E3);
    U[4] = cmul(C2, E4);
    U[5] = cmul(U[4], E1);
    U[6] = cmul(U[4], E2);
    U[7] = cmul(U[4], E3);
  }
  const cf F1 = cexp_p(ar, ai, 128.f);
  const cf F2 = cexp_p(ar, ai, 256.f);
  const cf F4 = cexp_p(ar, ai, 512.f);
  const cf F8 = cexp_p(ar, ai, 1024.f);
  const cf F3 = cmul(F2, F1);

  f32x4 acc[3];
#pragma unroll
  for (int mt = 0; mt < 3; ++mt) acc[mt] = (f32x4){0.f, 0.f, 0.f, 0.f};
  const int fr = lane & 15, fq = lane >> 4;

  auto do_mfma = [&](int mt) {
    asm volatile("" ::: "memory");
    __builtin_amdgcn_sched_barrier(0);
#pragma unroll
    for (int s = 0; s < 4; ++s) {
      const int off = fr * KG_RS + 32 * s + 8 * fq;
      bf16x8 ah = *reinterpret_cast<const bf16x8*>(Wh + off);
      bf16x8 al = *reinterpret_cast<const bf16x8*>(Wl + off);
      bf16x8 bh = *reinterpret_cast<const bf16x8*>(Vh + off);
      bf16x8 bl = *reinterpret_cast<const bf16x8*>(Vl + off);
      acc[mt] = __builtin_amdgcn_mfma_f32_16x16x32_bf16(ah, bh, acc[mt], 0, 0, 0);
      acc[mt] = __builtin_amdgcn_mfma_f32_16x16x32_bf16(ah, bl, acc[mt], 0, 0, 0);
      acc[mt] = __builtin_amdgcn_mfma_f32_16x16x32_bf16(al, bh, acc[mt], 0, 0, 0);
    }
    __builtin_amdgcn_sched_barrier(0);
    asm volatile("" ::: "memory");
  };
#pragma unroll
  for (int e = 0; e < 8; ++e) kg_store(Wh, Wl, e, lane, U[e].r, -U[e].i);
#pragma unroll
  for (int e = 0; e < 8; ++e) { cf t = cmul(F1, U[e]); kg_store(Wh, Wl, 8 + e, lane, t.r, -t.i); }
  STAMPK(0, 1);
  do_mfma(0);
  STAMPK(0, 2);
#pragma unroll
  for (int e = 0; e < 4; ++e) {
    cf t = cmul(F2, U[2 * e]); kg_store(Wh, Wl, e, lane, t.r, -t.i);
    cf t2 = cmul(F3, U[2 * e]); kg_store(Wh, Wl, 4 + e, lane, t2.r, -t2.i);
  }
  {
    cf G[4];
    G[0] = F4; G[1] = cmul(F4, F1); G[2] = cmul(F4, F2); G[3] = cmul(F4, F3);
#pragma unroll
    for (int a = 0; a < 4; ++a) {
      cf t = cmul(G[a], U[0]); kg_store(Wh, Wl, 8 + 2 * a, lane, t.r, -t.i);
      cf t2 = cmul(G[a], U[4]); kg_store(Wh, Wl, 8 + 2 * a + 1, lane, t2.r, -t2.i);
    }
    do_mfma(1);
    cf H0 = cmul(F8, U[0]);
    kg_store(Wh, Wl, 0, lane, H0.r, -H0.i);
    { cf t = cmul(H0, F1); kg_store(Wh, Wl, 1, lane, t.r, -t.i); }
    { cf t = cmul(H0, F2); kg_store(Wh, Wl, 2, lane, t.r, -t.i); }
    { cf t = cmul(H0, F3); kg_store(Wh, Wl, 3, lane, t.r, -t.i); }
#pragma unroll
    for (int a = 0; a < 4; ++a) { cf t = cmul(H0, G[a]); kg_store(Wh, Wl, 4 + a, lane, t.r, -t.i); }
    { cf t = cmul(H0, F8); kg_store(Wh, Wl, 8, lane, t.r, -t.i); }
#pragma unroll
    for (int r = 9; r < 16; ++r) {
      *reinterpret_cast<unsigned*>(Wh + r * KG_RS + 2 * lane) = 0u;
      *reinterpret_cast<unsigned*>(Wl + r * KG_RS + 2 * lane) = 0u;
    }
    do_mfma(2);
  }
  bsync();
  STAMPK(0, 3);
  float* red = reinterpret_cast<float*>(smem);
#pragma unroll
  for (int mt = 0; mt < 3; ++mt)
#pragma unroll
    for (int j = 0; j < 4; ++j) red[w * KTAB + (mt * 16 + fq * 4 + j) * 16 + fr] = acc[mt][j];
  bsync();
  float* KT = red + 4 * KTAB;
  float* RM = KT + KTAB;
  {
    float m = 0.f;
#pragma unroll
    for (int r = 0; r < 3; ++r) {
      int i = tid + r * 256;
      float v = (red[i] + red[KTAB + i]) + (red[2 * KTAB + i] + red[3 * KTAB + i]);
      KT[i] = v;
      if (i < 41 * 16) m = __builtin_fmaxf(m, __builtin_fabsf(v));
    }
#pragma unroll
    for (int o = 32; o > 0; o >>= 1) m = __builtin_fmaxf(m, shfl_xor_f(m, o, lane));
    if (lane == 0) RM[w] = m;
  }
  bsync();
  {
    float m = __builtin_fmaxf(__builtin_fmaxf(RM[0], RM[1]), __builtin_fmaxf(RM[2], RM[3]));
    int e = (int)((f2u(m) >> 23) & 0xff) - 127;
    if (m == 0.f || e > 100 || e < -100) e = 13;
    const float kscale = u2f((unsigned)(127 + 13 - e) << 23);
    if (tid == 0) kinvp[d] = u2f((unsigned)(127 - 13 + e) << 23);
    const int l0 = tid * 8;
    u32x4 hv;
#pragma unroll
    for (int j = 0; j < 4; ++j) {
      float v0 = KT[ktab_index(l0 + 2 * j)], v1 = KT[ktab_index(l0 + 2 * j + 1)];
      hv[j] = pk2h(v0 * kscale, v1 * kscale);
    }
    *reinterpret_cast<u32x4*>(kexp + (size_t)d * SL + l0) = hv;
  }
  STAMPK(0, 5);
}

constexpr int G1_BM = 128, G1_BN = 112, G1_BK = 32;
constexpr int G1_BLOCKS = (SL / G1_BM) * (FF / G1_BN);
constexpr int G1_ABYTES = G1_BM * G1_BK * 2;
constexpr int G1_BRS = 224;
constexpr int G1_STAGE = 16384;
constexpr int G1_NS = 5;
constexpr int CV_XS = 4160;
constexpr int CV_XBYTES = 8 * CV_XS;
constexpr int CV_KB = 80;
constexpr int CV_KPAD = 31;
constexpr int CV_KBYTES = (CV_KPAD + 64) * CV_KB;
constexpr int K1_LDS = G1_NS * G1_STAGE;

__global__ __launch_bounds__(256, 2) NOPK_ATTR void k1_gemm1_conv(
    const u16* __restrict__ xn, const u16* __restrict__ xnT, const u16* __restrict__ kexp,
    const float* __restrict__ kinvp, const u16* __restrict__ w1, const u16* __restrict__ b1, const u16* __restrict__ dskip,
    u16* __restrict__ hbuf, u16* __restrict__ sT) {
  extern __shared__ __attribute__((aligned(16))) char smem[];
  const int tid = TIDX, lane = tid & 63, w = tid >> 6;
  if (BIDX < G1_BLOCKS) {
    const int bid = BIDX;
    const int xcd = bid & 7, rest = bid >> 3;
    const int mt_i = xcd * 2 + (rest & 1);
    const int nt_i = rest >> 1;
    const int m0 = mt_i * G1_BM, n0 = nt_i * G1_BN;
    const int fr = lane & 15, fq = lane >> 4;
    STAMPK(1, 0);
    const u16* src[4];
    int kstep[4];
#pragma unroll
    for (int i = 0; i < 4; ++i) {
      int p = (4 * w + i) * 64 + lane;
      if (p < 512) {
        int row = p >> 2, ph = p & 3;
        int ch = ph ^ (((row >> 3) & 1) << 1);
        src[i] = xn + (size_t)(m0 + row) * DM + ch * 8;
        kstep[i] = G1_BK;
      } else {
        int c = p - 512; if (c >= 448) c -= 64;
        int kr = c / 14, ch = c - kr * 14;
        src[i] = w1 + (size_t)kr * FF + n0 + ch * 8;
        kstep[i] = G1_BK * FF;
      }
    }
    auto issue = [&](int kt) {
      char* base = smem + (kt % G1_NS) * G1_STAGE + (4 * w) * 1024;
#pragma unroll
      for (int i = 0; i < 4; ++i)
        __builtin_amdgcn_global_load_lds((const unsigned*)(src[i] + (size_t)kt * kstep[i]),
                                         (unsigned*)(base + i * 1024 + lane * 16), 16, 0, 0);
    };
    float bb[7][4];
#pragma unroll
    for (int j = 0; j < 7; ++j) {
      u32x2 t = *reinterpret_cast<const u32x2*>(b1 + n0 + j * 16 + fq * 4);
      bb[j][0] = lo_f(t[0]); bb[j][1] = hi_f(t[0]); bb[j][2] = lo_f(t[1]); bb[j][3] = hi_f(t[1]);
    }
    f32x4 acc[2][7];
#pragma unroll
    for (int i = 0; i < 2; ++i)
#pragma unroll
      for (int j = 0; j < 7; ++j) acc[i][j] = (f32x4){0.f, 0.f, 0.f, 0.f};
    constexpr int NKT = DM / G1_BK;
#pragma unroll
    for (int s = 0; s < G1_NS - 1; ++s) issue(s);
    const int q4 = (lane & 15) >> 2, p4 = lane & 3;
    auto step = [&](auto KT) {
      constexpr int kt = decltype(KT)::value;
      constexpr int rem = (NKT - 1 - kt) < (G1_NS - 2) ? (NKT - 1 - kt) : (G1_NS - 2);
      wait_vm<4 * rem>();
      wait_lgkm0();
      __builtin_amdgcn_s_barrier();
      if constexpr (kt == 0) STAMPK(1, 1);
      if constexpr (kt == 4) STAMPK(1, 2);
      if constexpr (kt == 9) STAMPK(1, 3);
      if constexpr (kt + G1_NS - 1 < NKT) issue(kt + G1_NS - 1);
      const char* A = smem + (kt % G1_NS) * G1_STAGE;
      const unsigned baddr = lds_addr(smem) + (kt % G1_NS) * G1_STAGE + G1_ABYTES + (fq * 8 + q4) * G1_BRS + p4 * 8;
      bf16x4 tl[7], th[7];
      auto rd = [&](auto J) {
        constexpr int j = decltype(J)::value;
        tl[j] = tr_read<j * 32>(baddr);
        th[j] = tr_read<j * 32 + 4 * G1_BRS>(baddr);
      };
      Unroll<0, 7>::run(rd);
      bf16x8 af[2];
#pragma unroll
      for (int i = 0; i < 2; ++i) {
        int row = w * 32 + i * 16 + fr;
        af[i] = *reinterpret_cast<const bf16x8*>(A + row * 64 + ((fq ^ (((row >> 3) & 1) << 1)) << 4));
      }
      asm volatile("s_waitcnt lgkmcnt(0)"
                   : "+v"(tl[0]), "+v"(tl[1]), "+v"(tl[2]), "+v"(tl[3]), "+v"(tl[4]), "+v"(tl[5]), "+v"(tl[6]),
                     "+v"(th[0]), "+v"(th[1]), "+v"(th[2]), "+v"(th[3]), "+v"(th[4]), "+v"(th[5]), "+v"(th[6]));
#pragma unroll
      for (int j = 0; j < 7; ++j) {
        bf16x8 bfr = __builtin_shufflevector(tl[j], th[j], 0, 1, 2, 3, 4, 5, 6, 7);
#pragma unroll
        for (int i = 0; i < 2; ++i)
          acc[i][j] = __builtin_amdgcn_mfma_f32_16x16x32_bf16(bfr, af[i], acc[i][j], 0, 0, 0);
      }
    };
    Unroll<0, NKT>::run(step);
    STAMPK(1, 4);
#pragma unroll
    for (int i = 0; i < 2; ++i) {
      u16* hrow = hbuf + (size_t)(m0 + w * 32 + i * 16 + fr) * FF + n0 + fq * 4;
#pragma unroll
      for (int j = 0; j < 7; ++j) {
        float hv[4];
#pragma unroll
        for (int r = 0; r < 4; ++r) {
          float t = rbf(rbf(acc[i][j][r]) + bb[j][r]);
          t = t > 0.f ? t : 0.f;
          hv[r] = t * t;
        }
        u32x2 pk;
        pk[0] = pk2bf(hv[0], hv[1]);
        pk[1] = pk2bf(hv[2], hv[3]);
        *reinterpret_cast<u32x2*>(hrow + j * 16) = pk;
      }
    }
    STAMPK(1, 5);
    return;
  }
  const int d = BIDX - G1_BLOCKS;
  char* XS = smem;
  char* KM = smem + CV_XBYTES;
  const u16* xrow = xnT + (size_t)d * SL;
  STAMPK(1, 0);
  const u32x4 kv = *reinterpret_cast<const u32x4*>(kexp + (size_t)d * SL + tid * 8);
  u32x4 cur[2], prv[2];
#pragma unroll
  for (int rep = 0; rep < 2; ++rep) {
    int gg = tid + rep * 256 - 3;
    cur[rep] = (u32x4){0u, 0u, 0u, 0u}; prv[rep] = (u32x4){0u, 0u, 0u, 0u};
    if (rep == 0 || tid < 4) {
      if (gg >= 0 && gg < 256) cur[rep] = *reinterpret_cast<const u32x4*>(xrow + gg * 8);
      if (gg >= 1 && gg <= 256) prv[rep] = *reinterpret_cast<const u32x4*>(xrow + (gg - 1) * 8);
    }
  }
  const u32x4 xv = *reinterpret_cast<const u32x4*>(xrow + tid * 8);
  const float dsk = bf2f(dskip[d]);
  const float kinv = kinvp[d];
  for (int i = tid; i < CV_KPAD * CV_KB / 16; i += 256)
    *reinterpret_cast<u32x4*>(KM + i * 16) = (u32x4){0u, 0u, 0u, 0u};
#pragma unroll
  for (int rep = 0; rep < 2; ++rep) {
    if (rep == 0 || tid < 4) {
      int gg = tid + rep * 256 - 3;
      int q = 256 - gg;
      unsigned Dw[8];
#pragma unroll
      for (int k = 0; k < 4; ++k) {
        Dw[k] = pk2h(lo_f(prv[rep][k]), hi_f(prv[rep][k]));
        Dw[4 + k] = pk2h(lo_f(cur[rep][k]), hi_f(cur[rep][k]));
      }
#pragma unroll
      for (int c = 0; c < 8; ++c) {
        u32x4 o;
#pragma unroll
        for (int k = 0; k < 4; ++k) {
          const int la = 8 + c - 2 * k;
          unsigned val;
          if (la & 1) {
            unsigned dd = Dw[(la - 1) >> 1];
            val = (dd >> 16) | (dd << 16);
          } else {
            val = (Dw[la >> 1] & 0xffffu) | (Dw[(la >> 1) - 1] & 0xffff0000u);
          }
          o[k] = val;
        }
        *reinterpret_cast<u32x4*>(XS + c * CV_XS + q * 16) = o;
      }
    }
  }
  {
    const int l0 = tid * 8;
    *reinterpret_cast<u32x4*>(KM + (CV_KPAD + (l0 >> 5)) * CV_KB + (l0 & 31) * 2) = kv;
  }
  bsync();
  STAMPK(1, 2);
  f32x16 acc0, acc1;
#pragma unroll
  for (int i = 0; i < 16; ++i) { acc0[i] = 0.f; acc1[i] = 0.f; }
  {
    const int t0 = lane & 31, hh = lane >> 5;
    const int c = t0 & 7;
    const char* abase = XS + c * CV_XS + 2 * (2048 - 8 * (t0 >> 3) + 8 * hh);
    const char* bbase = KM + (CV_KPAD + t0) * CV_KB + 16 * hh;
#pragma unroll 2
    for (int it = 0; it < 8; ++it) {
      const int dl = w + 4 * it;
#pragma unroll
      for (int ks = 0; ks < 2; ++ks) {
        f16x8 a0 = *reinterpret_cast<const f16x8*>(abase - 64 * dl + 32 * ks);
        f16x8 a1 = *reinterpret_cast<const f16x8*>(abase - 64 * (dl + 32) + 32 * ks);
        const char* b0p = bbase - dl * CV_KB + 32 * ks;
        f16x8 b0 = *reinterpret_cast<const f16x8*>(b0p);
        f16x8 b1v = *reinterpret_cast<const f16x8*>(b0p + 32 * CV_KB);
        acc0 = __builtin_amdgcn_mfma_f32_32x32x16_f16(a0, b0, acc0, 0, 0, 0);
        acc1 = __builtin_amdgcn_mfma_f32_32x32x16_f16(a0, b1v, acc1, 0, 0, 0);
        acc1 = __builtin_amdgcn_mfma_f32_32x32x16_f16(a1, b0, acc1, 0, 0, 0);
      }
    }
  }
  bsync();
  STAMPK(1, 3);
  {
    float* red = reinterpret_cast<float*>(smem);
    const int col = lane & 31, hh = lane >> 5;
#pragma unroll
    for (int g = 0; g < 4; ++g) {
      f32x4 v0 = (f32x4){acc0[4 * g], acc0[4 * g + 1], acc0[4 * g + 2], acc0[4 * g + 3]};
      f32x4 v1 = (f32x4){acc1[4 * g], acc1[4 * g + 1], acc1[4 * g + 2], acc1[4 * g + 3]};
      *reinterpret_cast<f32x4*>(red + w * 2048 + 32 * col + 8 * g + 4 * hh) = v0;
      *reinterpret_cast<f32x4*>(red + w * 2048 + 1024 + 32 * col + 8 * g + 4 * hh) = v1;
    }
    bsync();
    const int t = tid * 8;
    f32x4 y0 = (f32x4){0.f, 0.f, 0.f, 0.f}, y1 = (f32x4){0.f, 0.f, 0.f, 0.f};
#pragma unroll
    for (int ww = 0; ww < 4; ++ww) {
      y0 += *reinterpret_cast<const f32x4*>(red + ww * 2048 + t);
      y1 += *reinterpret_cast<const f32x4*>(red + ww * 2048 + t + 4);
    }
    u32x4 o;
#pragma unroll
    for (int j = 0; j < 4; ++j) {
      float ya = (j < 2 ? y0[2 * j] : y1[2 * j - 4]) * kinv;
      float yb = (j < 2 ? y0[2 * j + 1] : y1[2 * j - 3]) * kinv;
      o[j] = pk2bf(rbf(ya) + rbf(lo_f(xv[j]) * dsk), rbf(yb) + rbf(hi_f(xv[j]) * dsk));
    }
    *reinterpret_cast<u32x4*>(sT + (size_t)d * SL + t) = o;
  }
  STAMPK(1, 5);
}

constexpr int G2_BM = 64, G2_BN = 64, G2_BK = 64;
constexpr int G2_BLOCKS = (SL / G2_BM) * (DM / G2_BN);
constexpr int G2_ABYTES = G2_BM * G2_BK * 2;
constexpr int G2_STAGE = 16384;
constexpr int G2_NS = 8;
constexpr int K2_LDS = G2_NS * G2_STAGE;

__device__ __forceinline__ int g2_bswz(int r) { return (((r >> 1) & 1) << 1) | (((r >> 3) & 1) << 2); }

__global__ __launch_bounds__(256, 1) NOPK_ATTR void k2_gemm2(
    const u16* __restrict__ hbuf, const u16* __restrict__ w2, const u16* __restrict__ b2,
    const u16* __restrict__ u, const u16* __restrict__ sT, u16* __restrict__ out) {
  extern __shared__ __attribute__((aligned(16))) char smem[];
  const int tid = TIDX, lane = tid & 63, w = tid >> 6;
  const int bid = BIDX;
  const int xcd = bid & 7, rest = bid >> 3;
  const int mt_i = xcd * 4 + (rest & 3);
  const int nt_i = rest >> 2;
  const int m0 = mt_i * G2_BM, n0 = nt_i * G2_BN;
  const int fr = lane & 15, fq = lane >> 4;
  const int wm = w & 1, wn = w >> 1;
  STAMPK(2, 0);
  u32x2 e_u[2][2];
  u16 e_s[2][2][4];
#pragma unroll
  for (int i = 0; i < 2; ++i)
#pragma unroll
    for (int j = 0; j < 2; ++j) {
      const int m = m0 + wm * 32 + i * 16 + fr, n = n0 + wn * 32 + j * 16 + fq * 4;
      e_u[i][j] = *reinterpret_cast<const u32x2*>(u + (size_t)m * DM + n);
#pragma unroll
      for (int r = 0; r < 4; ++r) e_s[i][j][r] = sT[(size_t)(n + r) * SL + m];
    }
  float bb[2][4];
#pragma unroll
  for (int j = 0; j < 2; ++j) {
    u32x2 t = *reinterpret_cast<const u32x2*>(b2 + n0 + wn * 32 + j * 16 + fq * 4);
    bb[j][0] = lo_f(t[0]); bb[j][1] = hi_f(t[0]); bb[j][2] = lo_f(t[1]); bb[j][3] = hi_f(t[1]);
  }
  const u16* srcA[2];
  const u16* srcB[2];
#pragma unroll
  for (int i = 0; i < 2; ++i) {
    int p = (2 * w + i) * 64 + lane;
    int row = p >> 3, ph = p & 7;
    srcA[i] = hbuf + (size_t)(m0 + row) * FF + (ph ^ (row & 7)) * 8;
    srcB[i] = w2 + (size_t)row * DM + n0 + (ph ^ g2_bswz(row)) * 8;
  }
  auto issue = [&](int kt) {
    char* base = smem + (kt % G2_NS) * G2_STAGE + (2 * w) * 1024 + lane * 16;
#pragma unroll
    for (int i = 0; i < 2; ++i) {
      __builtin_amdgcn_global_load_lds((const unsigned*)(srcA[i] + kt * G2_BK), (unsigned*)(base + i * 1024), 16, 0, 0);
      __builtin_amdgcn_global_load_lds((const unsigned*)(srcB[i] + (size_t)kt * G2_BK * DM),
                                       (unsigned*)(base + G2_ABYTES + i * 1024), 16, 0, 0);
    }
  };
  f32x4 acc[2][2];
#pragma unroll
  for (int i = 0; i < 2; ++i)
#pragma unroll
    for (int j = 0; j < 2; ++j) acc[i][j] = (f32x4){0.f, 0.f, 0.f, 0.f};
  constexpr int NKT = FF / G2_BK;
#pragma unroll
  for (int s = 0; s < G2_NS - 1; ++s) issue(s);
  const int q4 = (lane & 15) >> 2, p4 = lane & 3;
  auto step = [&](auto KT) {
    constexpr int kt = decltype(KT)::value;
    constexpr int rem = (NKT - 1 - kt) < (G2_NS - 2) ? (NKT - 1 - kt) : (G2_NS - 2);
    wait_vm<4 * rem>();
    wait_lgkm0();
    __builtin_amdgcn_s_barrier();
    if constexpr (kt == 0) STAMPK(2, 1);
    if constexpr (kt == 7) STAMPK(2, 2);
    if constexpr (kt == 14) STAMPK(2, 3);
    if constexpr (kt == 21) STAMPK(2, 4);
    if constexpr (kt + G2_NS - 1 < NKT) issue(kt + G2_NS - 1);
    const char* A = smem + (kt % G2_NS) * G2_STAGE;
    const unsigned bbase = lds_addr(smem) + (kt % G2_NS) * G2_STAGE + G2_ABYTES;
    bf16x4 t[2][2][2];
#pragma unroll
    for (int j = 0; j < 2; ++j) {
#pragma unroll
      for (int hf = 0; hf < 2; ++hf) {
        const int cb = wn * 2 + j;
        const int r0 = fq * 8 + q4 + 4 * hf;
        const int ch = 2 * cb + (p4 >> 1);
        const unsigned a0 = bbase + r0 * 128 + ((ch ^ g2_bswz(r0)) << 4) + (p4 & 1) * 8;
        t[0][j][hf] = tr_read<0>(a0);
        t[1][j][hf] = tr_read<32 * 128>(a0);
      }
    }
    bf16x8 af[2][2];
#pragma unroll
    for (int ks = 0; ks < 2; ++ks)
#pragma unroll
      for (int i = 0; i < 2; ++i) {
        int row = wm * 32 + i * 16 + fr;
        int ch = ks * 4 + fq;
        af[ks][i] = *reinterpret_cast<const bf16x8*>(A + row * 128 + ((ch ^ (row & 7)) << 4));
      }
    asm volatile("s_waitcnt lgkmcnt(0)"
                 : "+v"(t[0][0][0]), "+v"(t[0][0][1]), "+v"(t[0][1][0]), "+v"(t[0][1][1]),
                   "+v"(t[1][0][0]), "+v"(t[1][0][1]), "+v"(t[1][1][0]), "+v"(t[1][1][1]));
#pragma unroll
    for (int ks = 0; ks < 2; ++ks)
#pragma unroll
      for (int j = 0; j < 2; ++j) {
        bf16x8 bfr = __builtin_shufflevector(t[ks][j][0], t[ks][j][1], 0, 1, 2, 3, 4, 5, 6, 7);
#pragma unroll
        for (int i = 0; i < 2; ++i)
          acc[i][j] = __builtin_amdgcn_mfma_f32_16x16x32_bf16(bfr, af[ks][i], acc[i][j], 0, 0, 0);
      }
  };
  Unroll<0, NKT>::run(step);
  STAMPK(2, 5);
#pragma unroll
  for (int i = 0; i < 2; ++i)
#pragma unroll
    for (int j = 0; j < 2; ++j) {
      const int m = m0 + wm * 32 + i * 16 + fr, n = n0 + wn * 32 + j * 16 + fq * 4;
      float o[4];
#pragma unroll
      for (int r = 0; r < 4; ++r) {
        float z = rbf(rbf(acc[i][j][r]) + bb[j][r]);
        float g = rbf(z / (1.0f + exp_acc(-z)));
        float xv = (r & 1) ? hi_f(e_u[i][j][r >> 1]) : lo_f(e_u[i][j][r >> 1]);
        o[r] = xv + rbf(bf2f(e_s[i][j][r]) * g);
      }
      u32x2 pk;
      pk[0] = pk2bf(o[0], o[1]);
      pk[1] = pk2bf(o[2], o[3]);
      *reinterpret_cast<u32x2*>(out + (size_t)m * DM + n) = pk;
    }
  STAMPK(2, 6);
}

#if defined(__HIP_DEVICE_COMPILE__)
#pragma clang attribute pop
#endif
extern "C" void kernel_launch(void* const* d_in, const int* in_sizes, int n_in,
                              void* d_out, int out_size, void* d_ws, size_t ws_size,
                              hipStream_t stream) {
  const u16* u = (const u16*)d_in[0];
  const u16* logA = (const u16*)d_in[1];
  const u16* Aim = (const u16*)d_in[2];
  const u16* Cre = (const u16*)d_in[3];
  const u16* Cim = (const u16*)d_in[4];
  const u16* logdt = (const u16*)d_in[5];
  const u16* dskip = (const u16*)d_in[6];
  const u16* w1 = (const u16*)d_in[8];
  const u16* b1 = (const u16*)d_in[9];
  const u16* w2 = (const u16*)d_in[10];
  const u16* b2 = (const u16*)d_in[11];
  u16* out = (u16*)d_out;
  char* ws = (char*)d_ws;
  u16* xn = (u16*)(ws);
  u16* xnT = (u16*)(ws + (2u << 20));
  u16* sT = (u16*)(ws + (4u << 20));
  u16* kexp = (u16*)(ws + (6u << 20));
  float* kinvp = (float*)(ws + (7u << 20) + (900u << 10));
  u16* hbuf = (u16*)(ws + (8u << 20));

  hipFuncSetAttribute((const void*)k0_ln_kgen, hipFuncAttributeMaxDynamicSharedMemorySize, K0_LDS);
  hipFuncSetAttribute((const void*)k1_gemm1_conv, hipFuncAttributeMaxDynamicSharedMemorySize, K1_LDS);
  hipFuncSetAttribute((const void*)k2_gemm2, hipFuncAttributeMaxDynamicSharedMemorySize, K2_LDS);
  k0_ln_kgen<<<LN_BLOCKS + DM, 256, K0_LDS, stream>>>(u, logA, Aim, Cre, Cim, logdt, xn, xnT, kexp, kinvp);
  k1_gemm1_conv<<<G1_BLOCKS + DM, 256, K1_LDS, stream>>>(xn, xnT, kexp, kinvp, w1, b1, dskip, hbuf, sT);
  k2_gemm2<<<G2_BLOCKS, 256, K2_LDS, stream>>>(hbuf, w2, b2, u, sT, out);
}
```

```cpp
#include <hip/hip_runtime.h>
#include <hip/hip_bf16.h>
#include <stdint.h>
#include <type_traits>

#if defined(__HIP_DEVICE_COMPILE__)
#pragma clang attribute push(__attribute__((target("no-packed-fp32-ops"))), apply_to = function)
#endif
typedef unsigned short u16;
typedef __attribute__((ext_vector_type(8))) short bf16x8;
typedef __attribute__((ext_vector_type(4))) short bf16x4;
typedef __attribute__((ext_vector_type(8))) _Float16 f16x8;
typedef __attribute__((ext_vector_type(2))) _Float16 f16x2;
typedef __attribute__((ext_vector_type(2))) __bf16 bf16x2;
typedef __attribute__((ext_vector_type(2))) float f32x2;
typedef __attribute__((ext_vector_type(4))) float f32x4;
typedef __attribute__((ext_vector_type(16))) float f32x16;
typedef __attribute__((ext_vector_type(4))) unsigned int u32x4;
typedef __attribute__((ext_vector_type(2))) unsigned int u32x2;
#define LDS_AS __attribute__((address_space(3)))

#define TIDX ((int)__builtin_amdgcn_workitem_id_x())
#define BIDX ((int)__builtin_amdgcn_workgroup_id_x())
__device__ __forceinline__ float u2f(unsigned v) { return __builtin_bit_cast(float, v); }
__device__ __forceinline__ unsigned f2u(float v) { return __builtin_bit_cast(unsigned, v); }
__device__ __forceinline__ void bsync() {
  __builtin_amdgcn_fence(__ATOMIC_RELEASE, "workgroup");
  __builtin_amdgcn_s_barrier();
  __builtin_amdgcn_fence(__ATOMIC_ACQUIRE, "workgroup");
}
__device__ __forceinline__ float shfl_xor_f(float v, int m, int lane) {
  return u2f((unsigned)__builtin_amdgcn_ds_bpermute((lane ^ m) << 2, (int)f2u(v)));
}
__device__ __forceinline__ float exp_acc(float x) {
  const float L2E_HI = 1.44269502162933349609375f, L2E_LO = 1.92596299112661746e-8f;
  float t = x * L2E_HI;
  float e = __builtin_fmaf(x, L2E_HI, -t) + x * L2E_LO;
  float r = __builtin_amdgcn_exp2f(t);
  return __builtin_fmaf(r, e * 0.693147180559945309f, r);
}

__device__ __forceinline__ float silu_f(float z) {
  float dn = 1.0f + exp_acc(-z);
  float r = __builtin_amdgcn_rcpf(dn);
  float q = z * r;
  float e = __builtin_fmaf(-q, dn, z);
  float res = __builtin_fmaf(e, r, q);
  return (dn < 3.0e38f) ? res : -0.0f * 0.f + (z * 0.f);
}

constexpr int SL = 2048;
constexpr int DM = 448;
constexpr int NMODE = 256;
constexpr int FF = 1792;
#define HPAD 64
constexpr int HS = FF + HPAD;
constexpr int KTAB = 48 * 16;

__device__ __forceinline__ float bf2f(u16 v) { return u2f(((unsigned)v) << 16); }
__device__ __forceinline__ unsigned pk2bf(float a, float b) {
  f32x2 v = {a, b};
  bf16x2 r = __builtin_convertvector(v, bf16x2);
  return *reinterpret_cast<unsigned*>(&r);
}
__device__ __forceinline__ u16 f2bf(float f) { return (u16)(pk2bf(f, 0.f) & 0xffffu); }
__device__ __forceinline__ float rbf(float f) { return u2f(pk2bf(f, 0.f) << 16); }
__device__ __forceinline__ float lo_f(unsigned v) { return u2f(v << 16); }
__device__ __forceinline__ float hi_f(unsigned v) { return u2f(v & 0xffff0000u); }
__device__ __forceinline__ unsigned pk2h(float a, float b) {
  f32x2 v = {a, b};
  f16x2 r = __builtin_convertvector(v, f16x2);
  return *reinterpret_cast<unsigned*>(&r);
}

template <int I, int N> struct Unroll {
  template <class F> static __device__ __forceinline__ void run(F& f) {
    f(std::integral_constant<int, I>{});
    Unroll<I + 1, N>::run(f);
  }
};
template <int N> struct Unroll<N, N> {
  template <class F> static __device__ __forceinline__ void run(F&) {}
};
template <int N> __device__ __forceinline__ void wait_vm() {
  asm volatile("s_waitcnt vmcnt(%0)" ::"n"(N) : "memory");
}
__device__ __forceinline__ void wait_lgkm0() { asm volatile("s_waitcnt lgkmcnt(0)" ::: "memory"); }

template <int OFF> __device__ __forceinline__ bf16x4 tr_read(unsigned addr) {
  bf16x4 r;
  asm volatile("ds_read_b64_tr_b16 %0, %1 offset:%2" : "=v"(r) : "v"(addr), "n"(OFF));
  return r;
}
__device__ __forceinline__ void wait14(bf16x4 (&a)[7], bf16x4 (&b)[7]) {
  asm volatile("s_waitcnt lgkmcnt(0)"
               : "+v"(a[0]), "+v"(a[1]), "+v"(a[2]), "+v"(a[3]), "+v"(a[4]), "+v"(a[5]), "+v"(a[6]),
                 "+v"(b[0]), "+v"(b[1]), "+v"(b[2]), "+v"(b[3]), "+v"(b[4]), "+v"(b[5]), "+v"(b[6]));
}
template <int OFF> __device__ __forceinline__ bf16x4 ld_b64(unsigned addr) {
  bf16x4 r;
  asm volatile("ds_read_b64 %0, %1 offset:%2" : "=v"(r) : "v"(addr), "n"(OFF));
  return r;
}
__device__ __forceinline__ unsigned lds_addr(const void* p) { return (unsigned)(size_t)p; }

#define STAMPK(k, s)
__device__ __forceinline__ u32x4 pair_cols(u32x2 a, u32x2 b) {
  auto r0 = __builtin_amdgcn_permlane16_swap(a[0], b[0], false, false);
  auto r1 = __builtin_amdgcn_permlane16_swap(a[1], b[1], false, false);
  return (u32x4){r0[0], r1[0], r0[1], r1[1]};
}
struct cf { float r, i; };
__device__ __forceinline__ cf cmul(cf a, cf b) {
  cf o; o.r = a.r * b.r - a.i * b.i; o.i = a.r * b.i + a.i * b.r; return o;
}
__device__ __forceinline__ cf cexp_p(float ar, float ai, float p) {
  float m = __builtin_amdgcn_exp2f((ar * p) * 1.4426950408889634f);
  float rev = (ai * p) * 0.15915494309189535f;
  rev = __builtin_amdgcn_fractf(rev);
  cf o; o.r = m * __builtin_amdgcn_cosf(rev); o.i = m * __builtin_amdgcn_sinf(rev); return o;
}
template <int CTRL> __device__ __forceinline__ float dpp_add(float s) {
  int t = __builtin_amdgcn_update_dpp(0, (int)f2u(s), CTRL, 0xf, 0xf, true);
  return s + u2f((unsigned)t);
}
__device__ __forceinline__ float red16(float s) {
  s = dpp_add<0xB1>(s);
  s = dpp_add<0x4E>(s);
  s = dpp_add<0x141>(s);
  s = dpp_add<0x140>(s);
  return s;
}

constexpr int LN_ROWS = 16;
constexpr int LN_BLOCKS = SL / LN_ROWS;
constexpr int KG_RS = 136;
constexpr int K0_LDS = 4 * 2 * 16 * KG_RS * 2 + 64;

__device__ __forceinline__ int ktab_index(int l) {
  int pos, r, b;
  if (l < 256) { pos = l; r = ((pos >> 7) << 3) + (pos & 7); b = (pos >> 3) & 15; return r * 16 + b; }
  pos = (int)rbf((float)l);
  if (pos < 512) { int q = pos - 256; r = 16 + ((q >> 7) << 2) + ((q >> 1) & 3); b = (q >> 3) & 15; }
  else if (pos < 1024) { int q = pos - 512; r = 24 + ((q >> 7) << 1) + ((q >> 2) & 1); b = (q >> 3) & 15; }
  else if (pos < 2048) { int q = pos - 1024; r = 32 + (q >> 7); b = (q >> 3) & 15; }
  else { r = 40; b = 0; }
  return r * 16 + b;
}

#define NOPK_ATTR
__global__ __launch_bounds__(256) NOPK_ATTR void k0_ln_kgen(
    const u16* __restrict__ u, const u16* __restrict__ logA, const u16* __restrict__ Aim,
    const u16* __restrict__ Cre, const u16* __restrict__ Cim, const u16* __restrict__ logdt,
    u16* __restrict__ xn, u16* __restrict__ xnT, u16* __restrict__ kexp, float* __restrict__ kinvp) {
  extern __shared__ __attribute__((aligned(16))) char smem[];
  const int tid = TIDX, lane = tid & 63, w = tid >> 6;
  if (BIDX < LN_BLOCKS) {
    u16* tile = reinterpret_cast<u16*>(smem);
    constexpr int TS = DM + 8;
    STAMPK(0, 0);
    const int rb = tid >> 4, l16 = tid & 15;
    const int row = BIDX * LN_ROWS + rb;
    u32x2 v[7];
#pragma unroll
    for (int i = 0; i < 7; ++i)
      v[i] = *reinterpret_cast<const u32x2*>(u + (size_t)row * DM + i * 64 + l16 * 4);
    float x[28];
#pragma unroll
    for (int i = 0; i < 7; ++i) {
      x[4 * i] = lo_f(v[i][0]); x[4 * i + 1] = hi_f(v[i][0]);
      x[4 * i + 2] = lo_f(v[i][1]); x[4 * i + 3] = hi_f(v[i][1]);
    }
    float s = 0.f;
#pragma unroll
    for (int j = 0; j < 28; ++j) s += x[j];
    s = red16(s);
    const float mean = s * (1.0f / DM);
    float q = 0.f;
#pragma unroll
    for (int j = 0; j < 28; ++j) { float dd = x[j] - mean; q += dd * dd; }
    q = red16(q);
    const float var_b = rbf(q * (1.0f / DM));
    const float mean_b = rbf(mean);
    const float den = rbf(__builtin_sqrtf(rbf(var_b + 1e-5f)));
#pragma unroll
    for (int i = 0; i < 7; ++i) {
      u32x2 o;
      o[0] = pk2bf(rbf(x[4 * i] - mean_b) / den, rbf(x[4 * i + 1] - mean_b) / den);
      o[1] = pk2bf(rbf(x[4 * i + 2] - mean_b) / den, rbf(x[4 * i + 3] - mean_b) / den);
      *reinterpret_cast<u32x2*>(xn + (size_t)row * DM + i * 64 + l16 * 4) = o;
      *reinterpret_cast<u32x2*>(tile + rb * TS + i * 64 + l16 * 4) = o;
    }
    bsync();
    STAMPK(0, 1);
    for (int d = tid; d < DM; d += 256) {
      unsigned pk[8];
#pragma unroll
      for (int r = 0; r < 8; ++r)
        pk[r] = (unsigned)tile[(2 * r) * TS + d] | ((unsigned)tile[(2 * r + 1) * TS + d] << 16);
      u32x4* dst = reinterpret_cast<u32x4*>(xnT + (size_t)d * SL + BIDX * LN_ROWS);
      dst[0] = (u32x4){pk[0], pk[1], pk[2], pk[3]};
      dst[1] = (u32x4){pk[4], pk[5], pk[6], pk[7]};
    }
    STAMPK(0, 5);
    return;
  }
  const int d = BIDX - LN_BLOCKS;
  u16* wbase = reinterpret_cast<u16*>(smem) + w * (2 * 16 * KG_RS);
  u16* Vb = wbase;
  u16* Wb = wbase + 16 * KG_RS;
  STAMPK(0, 0);
  const int idx = d * NMODE + tid;
  const u16 r_ldt = logdt[d], r_la = logA[idx], r_ai = Aim[idx], r_cr = Cre[idx], r_ci = Cim[idx];
  const float dt = rbf(exp_acc(bf2f(r_ldt)));
  const float Are = -rbf(exp_acc(bf2f(r_la)));
  const float ar = Are * dt;
  const float ai = bf2f(r_ai) * dt;
  cf C2; C2.r = 2.0f * (bf2f(r_cr) * dt); C2.i = 2.0f * (bf2f(r_ci) * dt);
  float* RM = reinterpret_cast<float*>(smem + 4 * 2 * 16 * KG_RS * 2);
  float wsc, wsc_inv;
  {
    float m = __builtin_fmaxf(__builtin_fabsf(C2.r), __builtin_fabsf(C2.i));
    float sm = __builtin_fabsf(C2.r) + __builtin_fabsf(C2.i);
#pragma unroll
    for (int o = 32; o > 0; o >>= 1) { m = __builtin_fmaxf(m, shfl_xor_f(m, o, lane)); sm += shfl_xor_f(sm, o, lane); }
    if (lane == 0) RM[w] = sm;
    int e = (int)((f2u(m) >> 23) & 0xff) - 127;
    if (m == 0.f || e > 100 || e < -100) e = 13;
    wsc = u2f((unsigned)(127 + 13 - e) << 23);
    wsc_inv = u2f((unsigned)(127 - 13 + e) << 23);
  }
  C2.r *= wsc; C2.i *= wsc;
  auto st = [&](u16* buf, int row, float re, float im) {
    *reinterpret_cast<unsigned*>(buf + row * KG_RS + 2 * lane) = pk2h(re, im);
  };
  {
    cf V1 = cexp_p(ar, ai, 8.f);
    cf V8 = cexp_p(ar, ai, 64.f);
    cf V[8];
    V[0].r = 1.f; V[0].i = 0.f;
    V[1] = V1;
    V[2] = cmul(V1, V1);
    V[3] = cmul(V[2], V1);
    V[4] = cmul(V[2], V[2]);
    V[5] = cmul(V[4], V1);
    V[6] = cmul(V[4], V[2]);
    V[7] = cmul(V[4], V[3]);
#pragma unroll
    for (int b = 0; b < 8; ++b) st(Vb, b, V[b].r, V[b].i);
    st(Vb, 8, V8.r, V8.i);
#pragma unroll
    for (int b = 1; b < 8; ++b) { cf t = cmul(V8, V[b]); st(Vb, 8 + b, t.r, t.i); }
  }
  cf U[8];
  {
    cf E1 = cexp_p(ar, ai, 1.f);
    cf E2 = cmul(E1, E1);
    cf E3 = cmul(E2, E1);
    cf E4 = cmul(E2, E2);
    U[0] = C2;
    U[1] = cmul(C2, E1);
    U[2] = cmul(C2, E2);
    U[3] = cmul(C2, E3);
    U[4] = cmul(C2, E4);
    U[5] = cmul(U[4], E1);
    U[6] = cmul(U[4], E2);
    U[7] = cmul(U[4], E3);
  }
  const cf F1 = cexp_p(ar, ai, 128.f);
  const cf F2 = cexp_p(ar, ai, 256.f);
  const cf F4 = cexp_p(ar, ai, 512.f);
  const cf F8 = cexp_p(ar, ai, 1024.f);
  const cf F3 = cmul(F2, F1);

  f32x4 acc[3];
#pragma unroll
  for (int mt = 0; mt < 3; ++mt) acc[mt] = (f32x4){0.f, 0.f, 0.f, 0.f};
  const int fr = lane & 15, fq = lane >> 4;
  asm volatile("" ::: "memory");
  f16x8 bv[4];
#pragma unroll
  for (int s4 = 0; s4 < 4; ++s4)
    bv[s4] = *reinterpret_cast<const f16x8*>(Vb + fr * KG_RS + 32 * s4 + 8 * fq);
  auto tile = [&](int mt, const cf* T) {
    asm volatile("" ::: "memory");
#pragma unroll
    for (int r = 0; r < 16; ++r) st(Wb, r, T[r].r, -T[r].i);
    asm volatile("" ::: "memory");
#pragma unroll
    for (int s4 = 0; s4 < 4; ++s4) {
      f16x8 a = *reinterpret_cast<const f16x8*>(Wb + fr * KG_RS + 32 * s4 + 8 * fq);
      acc[mt] = __builtin_amdgcn_mfma_f32_16x16x32_f16(a, bv[s4], acc[mt], 0, 0, 0);
    }
    asm volatile("" ::: "memory");
  };
  cf T[16];
#pragma unroll
  for (int e = 0; e < 8; ++e) { T[e] = U[e]; T[8 + e] = cmul(F1, U[e]); }
  STAMPK(0, 1);
  tile(0, T);
  STAMPK(0, 2);
  cf G[4];
  G[0] = F4; G[1] = cmul(F4, F1); G[2] = cmul(F4, F2); G[3] = cmul(F4, F3);
#pragma unroll
  for (int e = 0; e < 4; ++e) {
    T[e] = cmul(F2, U[2 * e]);
    T[4 + e] = cmul(F3, U[2 * e]);
    T[8 + 2 * e] = cmul(G[e], U[0]);
    T[8 + 2 * e + 1] = cmul(G[e], U[4]);
  }
  tile(1, T);
  {
    cf H0 = cmul(F8, U[0]);
    T[0] = H0; T[1] = cmul(H0, F1); T[2] = cmul(H0, F2); T[3] = cmul(H0, F3);
#pragma unroll
    for (int a2 = 0; a2 < 4; ++a2) T[4 + a2] = cmul(H0, G[a2]);
    T[8] = cmul(H0, F8);
#pragma unroll
    for (int r = 9; r < 16; ++r) { T[r].r = 0.f; T[r].i = 0.f; }
  }
  tile(2, T);
#pragma unroll
  for (int mt = 0; mt < 3; ++mt) acc[mt] *= wsc_inv;
  bsync();
  STAMPK(0, 3);
  float* red = reinterpret_cast<float*>(smem);
#pragma unroll
  for (int mt = 0; mt < 3; ++mt)
#pragma unroll
    for (int j = 0; j < 4; ++j) red[w * KTAB + (mt * 16 + fq * 4 + j) * 16 + fr] = acc[mt][j];
  bsync();
  float* KT = red + 4 * KTAB;
#pragma unroll
  for (int r = 0; r < 3; ++r) {
    int i = tid + r * 256;
    KT[i] = (red[i] + red[KTAB + i]) + (red[2 * KTAB + i] + red[3 * KTAB + i]);
  }
  bsync();
  {
    float m = (RM[0] + RM[1]) + (RM[2] + RM[3]);
    int e = (int)((f2u(m) >> 23) & 0xff) - 127;
    if (m == 0.f || e > 100 || e < -100) e = 14;
    const float kscale = u2f((unsigned)(127 + 14 - e) << 23);
    if (tid == 0) kinvp[d] = u2f((unsigned)(127 - 14 + e) << 23);
    const int l0 = tid * 8;
    u32x4 hv;
#pragma unroll
    for (int j = 0; j < 4; ++j) {
      float v0 = KT[ktab_index(l0 + 2 * j)], v1 = KT[ktab_index(l0 + 2 * j + 1)];
      hv[j] = pk2h(v0 * kscale, v1 * kscale);
    }
    *reinterpret_cast<u32x4*>(kexp + (size_t)d * SL + l0) = hv;
  }
  STAMPK(0, 5);
}

constexpr int G1_BM = 128, G1_BN = 112, G1_BK = 64;
constexpr int G1_BLOCKS = (SL / G1_BM) * (FF / G1_BN);
constexpr int G1_ABYTES = G1_BM * G1_BK * 2;
constexpr int G1_BRS = 224;
constexpr int G1_STAGE = 32768;
constexpr int G1_NS = 2;
constexpr int CV_XS = 4160;
constexpr int CV_XBYTES = 36864;
constexpr int CV_KB = 80;
constexpr int CV_KPAD = 31;
constexpr int CV_KBYTES = (CV_KPAD + 64) * CV_KB;
constexpr int K1_LDS = 81920;

__global__ __launch_bounds__(256, 2) NOPK_ATTR void k1_gemm1_conv(
    const u16* __restrict__ xn, const u16* __restrict__ xnT, const u16* __restrict__ kexp,
    const float* __restrict__ kinvp, const u16* __restrict__ w1, const u16* __restrict__ b1, const u16* __restrict__ dskip,
    u16* __restrict__ hbuf, u16* __restrict__ sT) {
  extern __shared__ __attribute__((aligned(16))) char smem[];
  const int tid = TIDX, lane = tid & 63, w = tid >> 6;
  if (BIDX < G1_BLOCKS) {
    const int bid = BIDX;
    const int xcd = bid & 7, rest = bid >> 3;
    const int mt_i = xcd * 2 + (rest & 1);
    const int nt_i = rest >> 1;
    const int m0 = mt_i * G1_BM, n0 = nt_i * G1_BN;
    const int fr = lane & 15, fq = lane >> 4;
    STAMPK(1, 0);
    const u16* src[8];
    int kstep[8];
#pragma unroll
    for (int i = 0; i < 8; ++i) {
      int p = (8 * w + i) * 64 + lane;
      if (p < 1024) {
        int row = p >> 3, ph = p & 7;
        src[i] = xn + (size_t)(m0 + row) * DM + (ph ^ (row & 7)) * 8;
        kstep[i] = G1_BK;
      } else {
        int c = p - 1024; if (c >= 896) c -= 128;
        int kr = c / 14, ch = c - kr * 14;
        src[i] = w1 + (size_t)kr * FF + n0 + ch * 8;
        kstep[i] = G1_BK * FF;
      }
    }
    auto issue = [&](int kt) {
      char* base = smem + (kt % G1_NS) * G1_STAGE + (8 * w) * 1024;
#pragma unroll
      for (int i = 0; i < 8; ++i)
        __builtin_amdgcn_global_load_lds((const unsigned*)(src[i] + (size_t)kt * kstep[i]),
                                         (unsigned*)(base + i * 1024 + lane * 16), 16, 0, 0);
    };
    float bb[7][4];
#pragma unroll
    for (int j = 0; j < 7; ++j) {
      u32x2 t = *reinterpret_cast<const u32x2*>(b1 + n0 + j * 16 + fq * 4);
      bb[j][0] = lo_f(t[0]); bb[j][1] = hi_f(t[0]); bb[j][2] = lo_f(t[1]); bb[j][3] = hi_f(t[1]);
    }
    f32x4 acc[2][7];
#pragma unroll
    for (int i = 0; i < 2; ++i)
#pragma unroll
      for (int j = 0; j < 7; ++j) acc[i][j] = (f32x4){0.f, 0.f, 0.f, 0.f};
    constexpr int NKT = DM / G1_BK;
    issue(0);
    issue(1);
    const int q4 = (lane & 15) >> 2, p4 = lane & 3;
    bf16x4 tl[2][2][7], th[2][2][7];
    bf16x8 af[2][2][2];
    auto rdfrag = [&](auto KT) {
      constexpr int kt = decltype(KT)::value;
      constexpr int st = kt & 1;
      const char* A = smem + (kt % G1_NS) * G1_STAGE;
      const unsigned baddr = lds_addr(smem) + (kt % G1_NS) * G1_STAGE + G1_ABYTES + (fq * 4 + q4) * G1_BRS + p4 * 8;
      auto rd = [&](auto J) {
        constexpr int j = decltype(J)::value;
        tl[st][0][j] = tr_read<j * 32>(baddr);
        th[st][0][j] = tr_read<j * 32 + 16 * G1_BRS>(baddr);
        tl[st][1][j] = tr_read<j * 32 + 32 * G1_BRS>(baddr);
        th[st][1][j] = tr_read<j * 32 + 48 * G1_BRS>(baddr);
      };
      Unroll<0, 7>::run(rd);
#pragma unroll
      for (int ks = 0; ks < 2; ++ks)
#pragma unroll
        for (int i = 0; i < 2; ++i) {
          int row = w * 32 + i * 16 + fr;
          int c0 = ks * 4 + (fq >> 1), c1 = c0 + 2;
          bf16x4 x0 = *reinterpret_cast<const bf16x4*>(A + row * 128 + ((c0 ^ (row & 7)) << 4) + (fq & 1) * 8);
          bf16x4 x1 = *reinterpret_cast<const bf16x4*>(A + row * 128 + ((c1 ^ (row & 7)) << 4) + (fq & 1) * 8);
          af[st][ks][i] = __builtin_shufflevector(x0, x1, 0, 1, 2, 3, 4, 5, 6, 7);
        }
    };
    auto waitfrag = [&](auto ST) {
      constexpr int st = decltype(ST)::value;
      wait14(tl[st][0], th[st][0]);
      wait14(tl[st][1], th[st][1]);
    };
    wait_vm<8>();
    __builtin_amdgcn_s_barrier();
    STAMPK(1, 1);
    rdfrag(std::integral_constant<int, 0>{});
    waitfrag(std::integral_constant<int, 0>{});
    auto step = [&](auto KT) {
      constexpr int kt = decltype(KT)::value;
      constexpr int st = kt & 1;
      if constexpr (kt + 1 < NKT) {
        wait_vm<0>();
        __builtin_amdgcn_s_barrier();
        if constexpr (kt == 2) STAMPK(1, 2);
        if constexpr (kt == 5) STAMPK(1, 3);
        if constexpr (kt + 2 < NKT) issue(kt + 2);
        rdfrag(std::integral_constant<int, kt + 1>{});
      }
#pragma unroll
      for (int ks = 0; ks < 2; ++ks)
#pragma unroll
        for (int j = 0; j < 7; ++j) {
          bf16x8 bfr = __builtin_shufflevector(tl[st][ks][j], th[st][ks][j], 0, 1, 2, 3, 4, 5, 6, 7);
#pragma unroll
          for (int i = 0; i < 2; ++i)
            acc[i][j] = __builtin_amdgcn_mfma_f32_16x16x32_bf16(bfr, af[st][ks][i], acc[i][j], 0, 0, 0);
        }
      if constexpr (kt + 1 < NKT) waitfrag(std::integral_constant<int, st ^ 1>{});
    };
    Unroll<0, NKT>::run(step);
    STAMPK(1, 4);
    u32x2 pk[2][7];
#pragma unroll
    for (int i = 0; i < 2; ++i) {
#pragma unroll
      for (int j = 0; j < 7; ++j) {
#pragma unroll
        for (int r = 0; r < 2; ++r) {
          unsigned p0 = pk2bf(acc[i][j][2 * r], acc[i][j][2 * r + 1]);
          unsigned p1 = pk2bf(lo_f(p0) + bb[j][2 * r], hi_f(p0) + bb[j][2 * r + 1]);
          float t0 = __builtin_fmaxf(lo_f(p1), 0.f), t1 = __builtin_fmaxf(hi_f(p1), 0.f);
          pk[i][j][r] = pk2bf(t0 * t0, t1 * t1);
        }
      }
    }
#pragma unroll
    for (int i = 0; i < 2; ++i) {
      u16* hrow = hbuf + (size_t)(m0 + w * 32 + i * 16 + fr) * HS + n0;
#pragma unroll
      for (int P = 0; P < 3; ++P)
        *reinterpret_cast<u32x4*>(hrow + P * 32 + (fq & 1) * 16 + (fq >> 1) * 8) = pair_cols(pk[i][2 * P], pk[i][2 * P + 1]);
      *reinterpret_cast<u32x2*>(hrow + 96 + fq * 4) = pk[i][6];
    }
    STAMPK(1, 5);
    return;
  }
  const int cb = BIDX - G1_BLOCKS;
  const int nch = (cb + 256 < DM) ? 2 : 1;
  char* XS = smem;
  char* KM = smem + CV_XBYTES;
  STAMPK(1, 0);
  u32x4 kv, cur[2], prv[2], xv;
  float dsk, kinv;
  auto prefetch = [&](int d) {
    const u16* xrow = xnT + (size_t)d * SL;
    kv = *reinterpret_cast<const u32x4*>(kexp + (size_t)d * SL + tid * 8);
#pragma unroll
    for (int rep = 0; rep < 2; ++rep) {
      int gg = tid + rep * 256 - 3;
      cur[rep] = (u32x4){0u, 0u, 0u, 0u}; prv[rep] = (u32x4){0u, 0u, 0u, 0u};
      if (rep == 0 || tid < 4) {
        if (gg >= 0 && gg < 256) cur[rep] = *reinterpret_cast<const u32x4*>(xrow + gg * 8);
        if (gg >= 1 && gg <= 256) prv[rep] = *reinterpret_cast<const u32x4*>(xrow + (gg - 1) * 8);
      }
    }
    xv = *reinterpret_cast<const u32x4*>(xrow + tid * 8);
    dsk = bf2f(dskip[d]);
    kinv = kinvp[d];
  };
  prefetch(cb);
  for (int i = tid; i < CV_KPAD * CV_KB / 16; i += 256)
    *reinterpret_cast<u32x4*>(KM + i * 16) = (u32x4){0u, 0u, 0u, 0u};
  for (int ch = 0; ch < nch; ++ch) {
    const int d = cb + ch * 256;
#pragma unroll
    for (int rep = 0; rep < 2; ++rep) {
      if (rep == 0 || tid < 4) {
        int gg = tid + rep * 256 - 3;
        int q = 256 - gg;
        unsigned Dw[8];
#pragma unroll
        for (int k = 0; k < 4; ++k) {
          Dw[k] = pk2h(lo_f(prv[rep][k]), hi_f(prv[rep][k]));
          Dw[4 + k] = pk2h(lo_f(cur[rep][k]), hi_f(cur[rep][k]));
        }
#pragma unroll
        for (int c = 0; c < 8; ++c) {
          u32x4 o;
#pragma unroll
          for (int k = 0; k < 4; ++k) {
            const int la = 8 + c - 2 * k;
            unsigned val;
            if (la & 1) {
              unsigned dd = Dw[(la - 1) >> 1];
              val = (dd >> 16) | (dd << 16);
            } else {
              val = (Dw[la >> 1] & 0xffffu) | (Dw[(la >> 1) - 1] & 0xffff0000u);
            }
            o[k] = val;
          }
          *reinterpret_cast<u32x4*>(XS + c * CV_XS + q * 16) = o;
        }
      }
    }
    {
      const int l0 = tid * 8;
      *reinterpret_cast<u32x4*>(KM + (CV_KPAD + (l0 >> 5)) * CV_KB + (l0 & 31) * 2) = kv;
    }
    const u32x4 xv_c = xv;
    const float dsk_c = dsk, kinv_c = kinv;
    bsync();
    if (ch == 0) STAMPK(1, 2);
    if (ch + 1 < nch) prefetch(d + 256);
    f32x16 acc0, acc1;
#pragma unroll
    for (int i = 0; i < 16; ++i) { acc0[i] = 0.f; acc1[i] = 0.f; }
    {
      const int t0 = lane & 31, hh = lane >> 5;
      const int c = t0 & 7;
      const char* abase = XS + c * CV_XS + 2 * (2048 - 8 * (t0 >> 3) + 8 * hh);
      const char* bbase = KM + (CV_KPAD + t0) * CV_KB + 16 * hh;
#pragma unroll 2
      for (int it = 0; it < 8; ++it) {
        const int dl = w + 4 * it;
#pragma unroll
        for (int ks = 0; ks < 2; ++ks) {
          f16x8 a0 = *reinterpret_cast<const f16x8*>(abase - 64 * dl + 32 * ks);
          f16x8 a1 = *reinterpret_cast<const f16x8*>(abase - 64 * (dl + 32) + 32 * ks);
          const char* b0p = bbase - dl * CV_KB + 32 * ks;
          f16x8 b0 = *reinterpret_cast<const f16x8*>(b0p);
          f16x8 b1v = *reinterpret_cast<const f16x8*>(b0p + 32 * CV_KB);
          acc0 = __builtin_amdgcn_mfma_f32_32x32x16_f16(a0, b0, acc0, 0, 0, 0);
          acc1 = __builtin_amdgcn_mfma_f32_32x32x16_f16(a0, b1v, acc1, 0, 0, 0);
          acc1 = __builtin_amdgcn_mfma_f32_32x32x16_f16(a1, b0, acc1, 0, 0, 0);
        }
      }
    }
    bsync();
    if (ch == 0) STAMPK(1, 3);
    float* red = reinterpret_cast<float*>(smem);
    {
      const int col = lane & 31, hh = lane >> 5;
#pragma unroll
      for (int g = 0; g < 4; ++g) {
        f32x4 v0 = (f32x4){acc0[4 * g], acc0[4 * g + 1], acc0[4 * g + 2], acc0[4 * g + 3]};
        f32x4 v1 = (f32x4){acc1[4 * g], acc1[4 * g + 1], acc1[4 * g + 2], acc1[4 * g + 3]};
        *reinterpret_cast<f32x4*>(red + w * 2304 + 36 * col + 8 * g + 4 * hh) = v0;
        *reinterpret_cast<f32x4*>(red + w * 2304 + 36 * (32 + col) + 8 * g + 4 * hh) = v1;
      }
    }
    bsync();
    {
      const int t = tid * 8;
      f32x4 y0 = (f32x4){0.f, 0.f, 0.f, 0.f}, y1 = (f32x4){0.f, 0.f, 0.f, 0.f};
#pragma unroll
      for (int ww = 0; ww < 4; ++ww) {
        y0 += *reinterpret_cast<const f32x4*>(red + ww * 2304 + 36 * (tid >> 2) + 8 * (tid & 3));
        y1 += *reinterpret_cast<const f32x4*>(red + ww * 2304 + 36 * (tid >> 2) + 8 * (tid & 3) + 4);
      }
      u32x4 o;
#pragma unroll
      for (int j = 0; j < 4; ++j) {
        float ya = (j < 2 ? y0[2 * j] : y1[2 * j - 4]) * kinv_c;
        float yb = (j < 2 ? y0[2 * j + 1] : y1[2 * j - 3]) * kinv_c;
        o[j] = pk2bf(rbf(ya) + rbf(lo_f(xv_c[j]) * dsk_c), rbf(yb) + rbf(hi_f(xv_c[j]) * dsk_c));
      }
      *reinterpret_cast<u32x4*>(sT + (size_t)d * SL + t) = o;
    }
    if (ch == 0) STAMPK(1, 4);
    if (ch + 1 < nch) bsync();
  }
  STAMPK(1, 5);
}

constexpr int G2_BM = 64, G2_BN = 64, G2_BK = 128;
constexpr int G2_BLOCKS = (SL / G2_BM) * (DM / G2_BN);
constexpr int G2_ABYTES = G2_BM * G2_BK * 2;
constexpr int G2_STAGE = 32768;
#define G2NS 4
constexpr int G2_NS = G2NS;
constexpr int K2_LDS = G2_NS * G2_STAGE;

__device__ __forceinline__ void wait8(bf16x4 (&a)[2][2][2]) {
  asm volatile("s_waitcnt lgkmcnt(0)"
               : "+v"(a[0][0][0]), "+v"(a[0][0][1]), "+v"(a[0][1][0]), "+v"(a[0][1][1]),
                 "+v"(a[1][0][0]), "+v"(a[1][0][1]), "+v"(a[1][1][0]), "+v"(a[1][1][1]));
}
__device__ __forceinline__ int g2_bswz(int r) { return ((r >> 1) & 3) << 1; }

__global__ __launch_bounds__(256, 1) NOPK_ATTR void k2_gemm2(
    const u16* __restrict__ hbuf, const u16* __restrict__ w2, const u16* __restrict__ b2,
    const u16* __restrict__ u, const u16* __restrict__ sT, u16* __restrict__ out) {
  extern __shared__ __attribute__((aligned(16))) char smem[];
  const int tid = TIDX, lane = tid & 63, w = tid >> 6;
  const int bid = BIDX;
  const int xcd = bid & 7, rest = bid >> 3;
  const int mt_i = xcd * 4 + (rest & 3);
  const int nt_i = rest >> 2;
  const int m0 = mt_i * G2_BM, n0 = nt_i * G2_BN;
  const int fr = lane & 15, fq = lane >> 4;
  const int wm = w & 1, wn = w >> 1;
  STAMPK(2, 0);
  const int ncol = wn * 32 + (fq & 1) * 16 + (fq >> 1) * 8;
  u32x4 e_u[2];
  u16 e_s[2][8];
#pragma unroll
  for (int i = 0; i < 2; ++i) {
    const int m = m0 + wm * 32 + i * 16 + fr, n = n0 + ncol;
    e_u[i] = *reinterpret_cast<const u32x4*>(u + (size_t)m * DM + n);
#pragma unroll
    for (int r = 0; r < 8; ++r) e_s[i][r] = sT[(size_t)(n + r) * SL + m];
  }
  float bb[2][4];
#pragma unroll
  for (int j = 0; j < 2; ++j) {
    u32x2 t = *reinterpret_cast<const u32x2*>(b2 + n0 + wn * 32 + j * 16 + fq * 4);
    bb[j][0] = lo_f(t[0]); bb[j][1] = hi_f(t[0]); bb[j][2] = lo_f(t[1]); bb[j][3] = hi_f(t[1]);
  }
  const u16* srcA[2];
  const u16* srcB[2];
#pragma unroll
  for (int i = 0; i < 2; ++i) {
    int p = (2 * w + i) * 64 + lane;
    int row = p >> 3, ph = p & 7;
    srcA[i] = hbuf + (size_t)(m0 + row) * HS + (ph ^ (row & 7)) * 8;
    srcB[i] = w2 + (size_t)row * DM + n0 + (ph ^ g2_bswz(row)) * 8;
  }
  auto issue = [&](int kt) {
    char* base = smem + (kt % G2_NS) * G2_STAGE + (2 * w) * 1024 + lane * 16;
#pragma unroll
    for (int hf = 0; hf < 2; ++hf)
#pragma unroll
      for (int i = 0; i < 2; ++i) {
        __builtin_amdgcn_global_load_lds((const unsigned*)(srcA[i] + kt * G2_BK + hf * 64),
                                         (unsigned*)(base + hf * 8192 + i * 1024), 16, 0, 0);
        __builtin_amdgcn_global_load_lds((const unsigned*)(srcB[i] + (size_t)(kt * G2_BK + hf * 64) * DM),
                                         (unsigned*)(base + G2_ABYTES + hf * 8192 + i * 1024), 16, 0, 0);
      }
  };
  f32x4 acc[2][2];
#pragma unroll
  for (int i = 0; i < 2; ++i)
#pragma unroll
    for (int j = 0; j < 2; ++j) acc[i][j] = (f32x4){0.f, 0.f, 0.f, 0.f};
  constexpr int NKT = FF / G2_BK;
#pragma unroll
  for (int s0 = 0; s0 < G2_NS - 1; ++s0) issue(s0);
  const int q4 = (lane & 15) >> 2, p4 = lane & 3;
  bf16x4 t[2][2][2][2][2];
  bf16x4 xa[2][2][2][2][2];
  auto rdfrag = [&](auto KT) {
    constexpr int kt = decltype(KT)::value;
    constexpr int st = kt & 1;
    const char* A = smem + (kt % G2_NS) * G2_STAGE;
    const unsigned bbase = lds_addr(smem) + (kt % G2_NS) * G2_STAGE + G2_ABYTES;
#pragma unroll
    for (int j = 0; j < 2; ++j)
#pragma unroll
      for (int hf = 0; hf < 2; ++hf) {
        const int r0 = fq * 4 + q4 + 16 * hf;
        const int ch = (wn * 2 + j) * 2 + (p4 >> 1);
        const unsigned a0 = bbase + r0 * 128 + ((ch ^ g2_bswz(r0)) << 4) + (p4 & 1) * 8;
        t[st][0][0][j][hf] = tr_read<0>(a0);
        t[st][0][1][j][hf] = tr_read<32 * 128>(a0);
        t[st][1][0][j][hf] = tr_read<64 * 128>(a0);
        t[st][1][1][j][hf] = tr_read<96 * 128>(a0);
      }
#pragma unroll
    for (int ks = 0; ks < 2; ++ks)
#pragma unroll
      for (int i = 0; i < 2; ++i)
#pragma unroll
        for (int hf = 0; hf < 2; ++hf) {
          int row = wm * 32 + i * 16 + fr;
          int c0 = ks * 4 + (fq >> 1) + 2 * hf;
          const unsigned a0 = lds_addr(A) + row * 128 + ((c0 ^ (row & 7)) << 4) + (fq & 1) * 8;
          xa[st][0][ks][i][hf] = ld_b64<0>(a0);
          xa[st][1][ks][i][hf] = ld_b64<8192>(a0);
        }
  };
  auto waitfrag = [&](auto ST) {
    constexpr int st = decltype(ST)::value;
    wait8(t[st][0]);
    wait8(t[st][1]);
    wait8(xa[st][0]);
    wait8(xa[st][1]);
  };
  wait_vm<8 * (G2_NS - 2)>();
  __builtin_amdgcn_s_barrier();
  STAMPK(2, 1);
  rdfrag(std::integral_constant<int, 0>{});
  waitfrag(std::integral_constant<int, 0>{});
  auto step = [&](auto KT) {
    constexpr int kt = decltype(KT)::value;
    constexpr int st = kt & 1;
    if constexpr (kt + 1 < NKT) {
      constexpr int last = (kt + G2_NS - 2) < (NKT - 1) ? (kt + G2_NS - 2) : (NKT - 1);
      wait_vm<8 * (last - (kt + 1))>();
      __builtin_amdgcn_s_barrier();
      if constexpr (kt == 3) STAMPK(2, 2);
      if constexpr (kt == 7) STAMPK(2, 3);
      if constexpr (kt == 11) STAMPK(2, 4);
      if constexpr (kt + G2_NS - 1 < NKT) issue(kt + G2_NS - 1);
      rdfrag(std::integral_constant<int, kt + 1>{});
    }
#pragma unroll
    for (int hv = 0; hv < 2; ++hv)
#pragma unroll
      for (int ks = 0; ks < 2; ++ks)
#pragma unroll
        for (int j = 0; j < 2; ++j) {
          bf16x8 bfr = __builtin_shufflevector(t[st][hv][ks][j][0], t[st][hv][ks][j][1], 0, 1, 2, 3, 4, 5, 6, 7);
#pragma unroll
          for (int i = 0; i < 2; ++i)
            acc[i][j] = __builtin_amdgcn_mfma_f32_16x16x32_bf16(
                bfr, __builtin_shufflevector(xa[st][hv][ks][i][0], xa[st][hv][ks][i][1], 0, 1, 2, 3, 4, 5, 6, 7),
                acc[i][j], 0, 0, 0);
        }
    if constexpr (kt + 1 < NKT) waitfrag(std::integral_constant<int, st ^ 1>{});
  };
  Unroll<0, NKT>::run(step);
  STAMPK(2, 5);
#pragma unroll
  for (int i = 0; i < 2; ++i) {
    const int m = m0 + wm * 32 + i * 16 + fr, n = n0 + ncol;
    u32x2 gp[2];
#pragma unroll
    for (int j = 0; j < 2; ++j) {
#pragma unroll
      for (int r = 0; r < 2; ++r) {
        unsigned p0 = pk2bf(acc[i][j][2 * r], acc[i][j][2 * r + 1]);
        unsigned p1 = pk2bf(lo_f(p0) + bb[j][2 * r], hi_f(p0) + bb[j][2 * r + 1]);
        float z0 = lo_f(p1), z1 = hi_f(p1);
        gp[j][r] = pk2bf(silu_f(z0), silu_f(z1));
      }
    }
    const u32x4 gv = pair_cols(gp[0], gp[1]);
    u32x4 o;
#pragma unroll
    for (int e = 0; e < 4; ++e)
      o[e] = pk2bf(lo_f(e_u[i][e]) + rbf(bf2f(e_s[i][2 * e]) * lo_f(gv[e])),
                   hi_f(e_u[i][e]) + rbf(bf2f(e_s[i][2 * e + 1]) * hi_f(gv[e])));
    *reinterpret_cast<u32x4*>(out + (size_t)m * DM + n) = o;
  }
  STAMPK(2, 6);
}

#if defined(__HIP_DEVICE_COMPILE__)
#pragma clang attribute pop
#endif
extern "C" void kernel_launch(void* const* d_in, const int* in_sizes, int n_in,
                              void* d_out, int out_size, void* d_ws, size_t ws_size,
                              hipStream_t stream) {
  const u16* u = (const u16*)d_in[0];
  const u16* logA = (const u16*)d_in[1];
  const u16* Aim = (const u16*)d_in[2];
  const u16* Cre = (const u16*)d_in[3];
  const u16* Cim = (const u16*)d_in[4];
  const u16* logdt = (const u16*)d_in[5];
  const u16* dskip = (const u16*)d_in[6];
  const u16* w1 = (const u16*)d_in[8];
  const u16* b1 = (const u16*)d_in[9];
  const u16* w2 = (const u16*)d_in[10];
  const u16* b2 = (const u16*)d_in[11];
  u16* out = (u16*)d_out;
  char* ws = (char*)d_ws;
  u16* xn = (u16*)(ws);
  u16* xnT = (u16*)(ws + (2u << 20));
  u16* sT = (u16*)(ws + (4u << 20));
  u16* kexp = (u16*)(ws + (6u << 20));
  float* kinvp = (float*)(ws + (7u << 20) + (900u << 10));
  u16* hbuf = (u16*)(ws + (8u << 20));

  hipFuncSetAttribute((const void*)k0_ln_kgen, hipFuncAttributeMaxDynamicSharedMemorySize, K0_LDS);
  hipFuncSetAttribute((const void*)k1_gemm1_conv, hipFuncAttributeMaxDynamicSharedMemorySize, K1_LDS);
  hipFuncSetAttribute((const void*)k2_gemm2, hipFuncAttributeMaxDynamicSharedMemorySize, K2_LDS);
  k0_ln_kgen<<<LN_BLOCKS + DM, 256, K0_LDS, stream>>>(u, logA, Aim, Cre, Cim, logdt, xn, xnT, kexp, kinvp);
  k1_gemm1_conv<<<G1_BLOCKS + 256, 256, K1_LDS, stream>>>(xn, xnT, kexp, kinvp, w1, b1, dskip, hbuf, sT);
  k2_gemm2<<<G2_BLOCKS, 256, K2_LDS, stream>>>(hbuf, w2, b2, u, sT, out);
}
```

```cpp
#include <hip/hip_runtime.h>
#include <hip/hip_bf16.h>
#include <stdint.h>
#include <type_traits>

#if defined(__HIP_DEVICE_COMPILE__)
#pragma clang attribute push(__attribute__((target("no-packed-fp32-ops"))), apply_to = function)
#endif
typedef unsigned short u16;
typedef __attribute__((ext_vector_type(8))) short bf16x8;
typedef __attribute__((ext_vector_type(4))) short bf16x4;
typedef __attribute__((ext_vector_type(8))) _Float16 f16x8;
typedef __attribute__((ext_vector_type(2))) _Float16 f16x2;
typedef __attribute__((ext_vector_type(2))) __bf16 bf16x2;
typedef __attribute__((ext_vector_type(2))) float f32x2;
typedef __attribute__((ext_vector_type(4))) float f32x4;
typedef __attribute__((ext_vector_type(16))) float f32x16;
typedef __attribute__((ext_vector_type(4))) unsigned int u32x4;
typedef __attribute__((ext_vector_type(2))) unsigned int u32x2;
#define LDS_AS __attribute__((address_space(3)))

#define TIDX ((int)__builtin_amdgcn_workitem_id_x())
#define BIDX ((int)__builtin_amdgcn_workgroup_id_x())
__device__ __forceinline__ float u2f(unsigned v) { return __builtin_bit_cast(float, v); }
__device__ __forceinline__ unsigned f2u(float v) { return __builtin_bit_cast(unsigned, v); }
__device__ __forceinline__ void bsync() {
  __builtin_amdgcn_fence(__ATOMIC_RELEASE, "workgroup");
  __builtin_amdgcn_s_barrier();
  __builtin_amdgcn_fence(__ATOMIC_ACQUIRE, "workgroup");
}
__device__ __forceinline__ float shfl_xor_f(float v, int m, int lane) {
  return u2f((unsigned)__builtin_amdgcn_ds_bpermute((lane ^ m) << 2, (int)f2u(v)));
}
__device__ __forceinline__ float exp_acc(float x) {
  const float L2E_HI = 1.44269502162933349609375f, L2E_LO = 1.92596299112661746e-8f;
  float t = x * L2E_HI;
  float e = __builtin_fmaf(x, L2E_HI, -t) + x * L2E_LO;
  float r = __builtin_amdgcn_exp2f(t);
  return __builtin_fmaf(r, e * 0.693147180559945309f, r);
}

__device__ __forceinline__ float silu_f(float z) {
  float dn = 1.0f + exp_acc(-z);
  float r = __builtin_amdgcn_rcpf(dn);
  float q = z * r;
  float e = __builtin_fmaf(-q, dn, z);
  float res = __builtin_fmaf(e, r, q);
  return (dn < 3.0e38f) ? res : -0.0f * 0.f + (z * 0.f);
}

constexpr int SL = 2048;
constexpr int DM = 448;
constexpr int NMODE = 256;
constexpr int FF = 1792;
#define HPAD 64
constexpr int HS = FF + HPAD;
constexpr int KTAB = 48 * 16;

__device__ __forceinline__ float bf2f(u16 v) { return u2f(((unsigned)v) << 16); }
__device__ __forceinline__ unsigned pk2bf(float a, float b) {
  f32x2 v = {a, b};
  bf16x2 r = __builtin_convertvector(v, bf16x2);
  return *reinterpret_cast<unsigned*>(&r);
}
__device__ __forceinline__ u16 f2bf(float f) { return (u16)(pk2bf(f, 0.f) & 0xffffu); }
__device__ __forceinline__ float rbf(float f) { return u2f(pk2bf(f, 0.f) << 16); }
__device__ __forceinline__ float lo_f(unsigned v) { return u2f(v << 16); }
__device__ __forceinline__ float hi_f(unsigned v) { return u2f(v & 0xffff0000u); }
__device__ __forceinline__ unsigned pk2h(float a, float b) {
  f32x2 v = {a, b};
  f16x2 r = __builtin_convertvector(v, f16x2);
  return *reinterpret_cast<unsigned*>(&r);
}

template <int I, int N> struct Unroll {
  template <class F> static __device__ __forceinline__ void run(F& f) {
    f(std::integral_constant<int, I>{});
    Unroll<I + 1, N>::run(f);
  }
};
template <int N> struct Unroll<N, N> {
  template <class F> static __device__ __forceinline__ void run(F&) {}
};
template <int N> __device__ __forceinline__ void wait_vm() {
  asm volatile("s_waitcnt vmcnt(%0)" ::"n"(N) : "memory");
}
__device__ __forceinline__ void wait_lgkm0() { asm volatile("s_waitcnt lgkmcnt(0)" ::: "memory"); }

template <int OFF> __device__ __forceinline__ bf16x4 tr_read(unsigned addr) {
  bf16x4 r;
  asm volatile("ds_read_b64_tr_b16 %0, %1 offset:%2" : "=v"(r) : "v"(addr), "n"(OFF));
  return r;
}
__device__ __forceinline__ void wait14(bf16x4 (&a)[7], bf16x4 (&b)[7]) {
  asm volatile("s_waitcnt lgkmcnt(0)"
               : "+v"(a[0]), "+v"(a[1]), "+v"(a[2]), "+v"(a[3]), "+v"(a[4]), "+v"(a[5]), "+v"(a[6]),
                 "+v"(b[0]), "+v"(b[1]), "+v"(b[2]), "+v"(b[3]), "+v"(b[4]), "+v"(b[5]), "+v"(b[6]));
}
template <int OFF> __device__ __forceinline__ bf16x4 ld_b64(unsigned addr) {
  bf16x4 r;
  asm volatile("ds_read_b64 %0, %1 offset:%2" : "=v"(r) : "v"(addr), "n"(OFF));
  return r;
}
__device__ __forceinline__ unsigned lds_addr(const void* p) { return (unsigned)(size_t)p; }

#define STAMPK(k, s)
__device__ __forceinline__ u32x4 pair_cols(u32x2 a, u32x2 b) {
  auto r0 = __builtin_amdgcn_permlane16_swap(a[0], b[0], false, false);
  auto r1 = __builtin_amdgcn_permlane16_swap(a[1], b[1], false, false);
  return (u32x4){r0[0], r1[0], r0[1], r1[1]};
}
struct cf { float r, i; };
__device__ __forceinline__ cf cmul(cf a, cf b) {
  cf o; o.r = a.r * b.r - a.i * b.i; o.i = a.r * b.i + a.i * b.r; return o;
}
__device__ __forceinline__ cf cexp_p(float ar, float ai, float p) {
  float m = __builtin_amdgcn_exp2f((ar * p) * 1.4426950408889634f);
  float rev = (ai * p) * 0.15915494309189535f;
  rev = __builtin_amdgcn_fractf(rev);
  cf o; o.r = m * __builtin_amdgcn_cosf(rev); o.i = m * __builtin_amdgcn_sinf(rev); return o;
}
template <int CTRL> __device__ __forceinline__ float dpp_add(float s) {
  int t = __builtin_amdgcn_update_dpp(0, (int)f2u(s), CTRL, 0xf, 0xf, true);
  return s + u2f((unsigned)t);
}
__device__ __forceinline__ float red16(float s) {
  s = dpp_add<0xB1>(s);
  s = dpp_add<0x4E>(s);
  s = dpp_add<0x141>(s);
  s = dpp_add<0x140>(s);
  return s;
}

constexpr int LN_ROWS = 16;
constexpr int LN_BLOCKS = SL / LN_ROWS;
constexpr int KG_RS = 136;
constexpr int K0_LDS = 4 * 2 * 16 * KG_RS * 2 + 64;

__device__ __forceinline__ int ktab_index(int l) {
  int pos, r, b;
  if (l < 256) { pos = l; r = ((pos >> 7) << 3) + (pos & 7); b = (pos >> 3) & 15; return r * 16 + b; }
  pos = (int)rbf((float)l);
  if (pos < 512) { int q = pos - 256; r = 16 + ((q >> 7) << 2) + ((q >> 1) & 3); b = (q >> 3) & 15; }
  else if (pos < 1024) { int q = pos - 512; r = 24 + ((q >> 7) << 1) + ((q >> 2) & 1); b = (q >> 3) & 15; }
  else if (pos < 2048) { int q = pos - 1024; r = 32 + (q >> 7); b = (q >> 3) & 15; }
  else { r = 40; b = 0; }
  return r * 16 + b;
}

#define NOPK_ATTR
__global__ __launch_bounds__(256) NOPK_ATTR void k0_ln_kgen(
    const u16* __restrict__ u, const u16* __restrict__ logA, const u16* __restrict__ Aim,
    const u16* __restrict__ Cre, const u16* __restrict__ Cim, const u16* __restrict__ logdt,
    u16* __restrict__ xn, u16* __restrict__ xnT, u16* __restrict__ kexp, float* __restrict__ kinvp) {
  extern __shared__ __attribute__((aligned(16))) char smem[];
  const int tid = TIDX, lane = tid & 63, w = tid >> 6;
  if (BIDX < LN_BLOCKS) {
    u16* tile = reinterpret_cast<u16*>(smem);
    constexpr int TS = DM + 8;
    STAMPK(0, 0);
    const int rb = tid >> 4, l16 = tid & 15;
    const int row = BIDX * LN_ROWS + rb;
    u32x2 v[7];
#pragma unroll
    for (int i = 0; i < 7; ++i)
      v[i] = *reinterpret_cast<const u32x2*>(u + (size_t)row * DM + i * 64 + l16 * 4);
    float x[28];
#pragma unroll
    for (int i = 0; i < 7; ++i) {
      x[4 * i] = lo_f(v[i][0]); x[4 * i + 1] = hi_f(v[i][0]);
      x[4 * i + 2] = lo_f(v[i][1]); x[4 * i + 3] = hi_f(v[i][1]);
    }
    float s = 0.f;
#pragma unroll
    for (int j = 0; j < 28; ++j) s += x[j];
    s = red16(s);
    const float mean = s * (1.0f / DM);
    float q = 0.f;
#pragma unroll
    for (int j = 0; j < 28; ++j) { float dd = x[j] - mean; q += dd * dd; }
    q = red16(q);
    const float var_b = rbf(q * (1.0f / DM));
    const float mean_b = rbf(mean);
    const float den = rbf(__builtin_sqrtf(rbf(var_b + 1e-5f)));
#pragma unroll
    for (int i = 0; i < 7; ++i) {
      u32x2 o;
      o[0] = pk2bf(rbf(x[4 * i] - mean_b) / den, rbf(x[4 * i + 1] - mean_b) / den);
      o[1] = pk2bf(rbf(x[4 * i + 2] - mean_b) / den, rbf(x[4 * i + 3] - mean_b) / den);
      *reinterpret_cast<u32x2*>(xn + (size_t)row * DM + i * 64 + l16 * 4) = o;
      *reinterpret_cast<u32x2*>(tile + rb * TS + i * 64 + l16 * 4) = o;
    }
    bsync();
    STAMPK(0, 1);
    for (int d = tid; d < DM; d += 256) {
      unsigned pk[8];
#pragma unroll
      for (int r = 0; r < 8; ++r)
        pk[r] = (unsigned)tile[(2 * r) * TS + d] | ((unsigned)tile[(2 * r + 1) * TS + d] << 16);
      u32x4* dst = reinterpret_cast<u32x4*>(xnT + (size_t)d * SL + BIDX * LN_ROWS);
      dst[0] = (u32x4){pk[0], pk[1], pk[2], pk[3]};
      dst[1] = (u32x4){pk[4], pk[5], pk[6], pk[7]};
    }
    STAMPK(0, 5);
    return;
  }
  const int d = BIDX - LN_BLOCKS;
  u16* wbase = reinterpret_cast<u16*>(smem) + w * (2 * 16 * KG_RS);
  u16* Vb = wbase;
  u16* Wb = wbase + 16 * KG_RS;
  STAMPK(0, 0);
  const int idx = d * NMODE + tid;
  const u16 r_ldt = logdt[d], r_la = logA[idx], r_ai = Aim[idx], r_cr = Cre[idx], r_ci = Cim[idx];
  const float dt = rbf(exp_acc(bf2f(r_ldt)));
  const float Are = -rbf(exp_acc(bf2f(r_la)));
  const float ar = Are * dt;
  const float ai = bf2f(r_ai) * dt;
  cf C2; C2.r = 2.0f * (bf2f(r_cr) * dt); C2.i = 2.0f * (bf2f(r_ci) * dt);
  float* RM = reinterpret_cast<float*>(smem + 4 * 2 * 16 * KG_RS * 2);
  float wsc, wsc_inv;
  {
    float m = __builtin_fmaxf(__builtin_fabsf(C2.r), __builtin_fabsf(C2.i));
    float sm = __builtin_fabsf(C2.r) + __builtin_fabsf(C2.i);
#pragma unroll
    for (int o = 32; o > 0; o >>= 1) { m = __builtin_fmaxf(m, shfl_xor_f(m, o, lane)); sm += shfl_xor_f(sm, o, lane); }
    if (lane == 0) RM[w] = sm;
    int e = (int)((f2u(m) >> 23) & 0xff) - 127;
    if (m == 0.f || e > 100 || e < -100) e = 13;
    wsc = u2f((unsigned)(127 + 13 - e) << 23);
    wsc_inv = u2f((unsigned)(127 - 13 + e) << 23);
  }
  C2.r *= wsc; C2.i *= wsc;
  auto st = [&](u16* buf, int row, float re, float im) {
    *reinterpret_cast<unsigned*>(buf + row * KG_RS + 2 * lane) = pk2h(re, im);
  };
  {
    cf V1 = cexp_p(ar, ai, 8.f);
    cf V8 = cexp_p(ar, ai, 64.f);
    cf V[8];
    V[0].r = 1.f; V[0].i = 0.f;
    V[1] = V1;
    V[2] = cmul(V1, V1);
    V[3] = cmul(V[2], V1);
    V[4] = cmul(V[2], V[2]);
    V[5] = cmul(V[4], V1);
    V[6] = cmul(V[4], V[2]);
    V[7] = cmul(V[4], V[3]);
#pragma unroll
    for (int b = 0; b < 8; ++b) st(Vb, b, V[b].r, V[b].i);
    st(Vb, 8, V8.r, V8.i);
#pragma unroll
    for (int b = 1; b < 8; ++b) { cf t = cmul(V8, V[b]); st(Vb, 8 + b, t.r, t.i); }
  }
  cf U[8];
  {
    cf E1 = cexp_p(ar, ai, 1.f);
    cf E2 = cmul(E1, E1);
    cf E3 = cmul(E2, E1);
    cf E4 = cmul(E2, E2);
    U[0] = C2;
    U[1] = cmul(C2, E1);
    U[2] = cmul(C2, E2);
    U[3] = cmul(C2, E3);
    U[4] = cmul(C2, E4);
    U[5] = cmul(U[4], E1);
    U[6] = cmul(U[4], E2);
    U[7] = cmul(U[4], E3);
  }
  const cf F1 = cexp_p(ar, ai, 128.f);
  const cf F2 = cexp_p(ar, ai, 256.f);
  const cf F4 = cexp_p(ar, ai, 512.f);
  const cf F8 = cexp_p(ar, ai, 1024.f);
  const cf F3 = cmul(F2, F1);

  f32x4 acc[3];
#pragma unroll
  for (int mt = 0; mt < 3; ++mt) acc[mt] = (f32x4){0.f, 0.f, 0.f, 0.f};
  const int fr = lane & 15, fq = lane >> 4;
  asm volatile("" ::: "memory");
  f16x8 bv[4];
#pragma unroll
  for (int s4 = 0; s4 < 4; ++s4)
    bv[s4] = *reinterpret_cast<const f16x8*>(Vb + fr * KG_RS + 32 * s4 + 8 * fq);
  auto tile = [&](int mt, const cf* T) {
    asm volatile("" ::: "memory");
#pragma unroll
    for (int r = 0; r < 16; ++r) st(Wb, r, T[r].r, -T[r].i);
    asm volatile("" ::: "memory");
#pragma unroll
    for (int s4 = 0; s4 < 4; ++s4) {
      f16x8 a = *reinterpret_cast<const f16x8*>(Wb + fr * KG_RS + 32 * s4 + 8 * fq);
      acc[mt] = __builtin_amdgcn_mfma_f32_16x16x32_f16(a, bv[s4], acc[mt], 0, 0, 0);
    }
    asm volatile("" ::: "memory");
  };
  cf T[16];
#pragma unroll
  for (int e = 0; e < 8; ++e) { T[e] = U[e]; T[8 + e] = cmul(F1, U[e]); }
  STAMPK(0, 1);
  tile(0, T);
  STAMPK(0, 2);
  cf G[4];
  G[0] = F4; G[1] = cmul(F4, F1); G[2] = cmul(F4, F2); G[3] = cmul(F4, F3);
#pragma unroll
  for (int e = 0; e < 4; ++e) {
    T[e] = cmul(F2, U[2 * e]);
    T[4 + e] = cmul(F3, U[2 * e]);
    T[8 + 2 * e] = cmul(G[e], U[0]);
    T[8 + 2 * e + 1] = cmul(G[e], U[4]);
  }
  tile(1, T);
  {
    cf H0 = cmul(F8, U[0]);
    T[0] = H0; T[1] = cmul(H0, F1); T[2] = cmul(H0, F2); T[3] = cmul(H0, F3);
#pragma unroll
    for (int a2 = 0; a2 < 4; ++a2) T[4 + a2] = cmul(H0, G[a2]);
    T[8] = cmul(H0, F8);
#pragma unroll
    for (int r = 9; r < 16; ++r) { T[r].r = 0.f; T[r].i = 0.f; }
  }
  tile(2, T);
#pragma unroll
  for (int mt = 0; mt < 3; ++mt) acc[mt] *= wsc_inv;
  bsync();
  STAMPK(0, 3);
  float* red = reinterpret_cast<float*>(smem);
#pragma unroll
  for (int mt = 0; mt < 3; ++mt)
#pragma unroll
    for (int j = 0; j < 4; ++j) red[w * KTAB + (mt * 16 + fq * 4 + j) * 16 + fr] = acc[mt][j];
  bsync();
  float* KT = red + 4 * KTAB;
#pragma unroll
  for (int r = 0; r < 3; ++r) {
    int i = tid + r * 256;
    KT[i] = (red[i] + red[KTAB + i]) + (red[2 * KTAB + i] + red[3 * KTAB + i]);
  }
  bsync();
  {
    float m = (RM[0] + RM[1]) + (RM[2] + RM[3]);
    int e = (int)((f2u(m) >> 23) & 0xff) - 127;
    if (m == 0.f || e > 100 || e < -100) e = 14;
    const float kscale = u2f((unsigned)(127 + 14 - e) << 23);
    if (tid == 0) kinvp[d] = u2f((unsigned)(127 - 14 + e) << 23);
    const int l0 = tid * 8;
    u32x4 hv;
#pragma unroll
    for (int j = 0; j < 4; ++j) {
      float v0 = KT[ktab_index(l0 + 2 * j)], v1 = KT[ktab_index(l0 + 2 * j + 1)];
      hv[j] = pk2h(v0 * kscale, v1 * kscale);
    }
    *reinterpret_cast<u32x4*>(kexp + (size_t)d * SL + l0) = hv;
  }
  STAMPK(0, 5);
}

constexpr int G1_BM = 128, G1_BN = 112, G1_BK = 64;
constexpr int G1_BLOCKS = (SL / G1_BM) * (FF / G1_BN);
constexpr int G1_ABYTES = G1_BM * G1_BK * 2;
constexpr int G1_BRS = 224;
constexpr int G1_STAGE = 32768;
constexpr int G1_NS = 2;
constexpr int CV_XS = 4160;
constexpr int CV_XBYTES = 36864;
constexpr int CV_KB = 80;
constexpr int CV_KPAD = 31;
constexpr int CV_KBYTES = (CV_KPAD + 64) * CV_KB;
constexpr int K1_LDS = 81920;

__global__ __launch_bounds__(256, 2) NOPK_ATTR void k1_gemm1_conv(
    const u16* __restrict__ xn, const u16* __restrict__ xnT, const u16* __restrict__ kexp,
    const float* __restrict__ kinvp, const u16* __restrict__ w1, const u16* __restrict__ b1, const u16* __restrict__ dskip,
    u16* __restrict__ hbuf, u16* __restrict__ sT) {
  extern __shared__ __attribute__((aligned(16))) char smem[];
  const int tid = TIDX, lane = tid & 63, w = tid >> 6;
  if (BIDX < G1_BLOCKS) {
    const int bid = BIDX;
    const int xcd = bid & 7, rest = bid >> 3;
    const int mt_i = xcd * 2 + (rest & 1);
    const int nt_i = rest >> 1;
    const int m0 = mt_i * G1_BM, n0 = nt_i * G1_BN;
    const int fr = lane & 15, fq = lane >> 4;
    STAMPK(1, 0);
    const int wu = __builtin_amdgcn_readfirstlane(w);
    const char* gbase = (wu < 2) ? reinterpret_cast<const char*>(xn) : reinterpret_cast<const char*>(w1);
    const unsigned kstepb = (wu < 2) ? (unsigned)(G1_BK * 2) : (unsigned)(G1_BK * FF * 2);
    unsigned off[8];
#pragma unroll
    for (int i = 0; i < 8; ++i) {
      int p = (8 * w + i) * 64 + lane;
      if (wu < 2) {
        int row = p >> 3, ph = p & 7;
        off[i] = (unsigned)(((m0 + row) * DM + (ph ^ (row & 7)) * 8) * 2);
      } else {
        int c = p - 1024; if (c >= 896) c -= 128;
        int kr = c / 14, ch = c - kr * 14;
        off[i] = (unsigned)((kr * FF + n0 + ch * 8) * 2);
      }
    }
    auto issue = [&](int kt) {
      char* base = smem + (kt & 1) * G1_STAGE + (8 * w) * 1024;
      const char* g = gbase + (size_t)kt * kstepb;
#pragma unroll
      for (int i = 0; i < 8; ++i)
        __builtin_amdgcn_global_load_lds((const unsigned*)(g + off[i]),
                                         (unsigned*)(base + i * 1024 + lane * 16), 16, 0, 0);
    };
    u32x2 bbp[7];
#pragma unroll
    for (int j = 0; j < 7; ++j) bbp[j] = *reinterpret_cast<const u32x2*>(b1 + n0 + j * 16 + fq * 4);
    f32x4 acc[2][7];
#pragma unroll
    for (int i = 0; i < 2; ++i)
#pragma unroll
      for (int j = 0; j < 7; ++j) acc[i][j] = (f32x4){0.f, 0.f, 0.f, 0.f};
    constexpr int NKT = DM / G1_BK;
    issue(0);
    issue(1);
    const int q4 = (lane & 15) >> 2, p4 = lane & 3;
    bf16x4 tl[2][2][7], th[2][2][7];
    bf16x8 af[2][2][2];
    auto rdfrag = [&](auto ST) {
      constexpr int st = decltype(ST)::value;
      const char* A = smem + st * G1_STAGE;
      const unsigned baddr = lds_addr(smem) + st * G1_STAGE + G1_ABYTES + (fq * 4 + q4) * G1_BRS + p4 * 8;
      auto rd = [&](auto J) {
        constexpr int j = decltype(J)::value;
        tl[st][0][j] = tr_read<j * 32>(baddr);
        th[st][0][j] = tr_read<j * 32 + 16 * G1_BRS>(baddr);
        tl[st][1][j] = tr_read<j * 32 + 32 * G1_BRS>(baddr);
        th[st][1][j] = tr_read<j * 32 + 48 * G1_BRS>(baddr);
      };
      Unroll<0, 7>::run(rd);
#pragma unroll
      for (int ks = 0; ks < 2; ++ks)
#pragma unroll
        for (int i = 0; i < 2; ++i) {
          int row = w * 32 + i * 16 + fr;
          int c0 = ks * 4 + (fq >> 1), c1 = c0 + 2;
          bf16x4 x0 = *reinterpret_cast<const bf16x4*>(A + row * 128 + ((c0 ^ (row & 7)) << 4) + (fq & 1) * 8);
          bf16x4 x1 = *reinterpret_cast<const bf16x4*>(A + row * 128 + ((c1 ^ (row & 7)) << 4) + (fq & 1) * 8);
          af[st][ks][i] = __builtin_shufflevector(x0, x1, 0, 1, 2, 3, 4, 5, 6, 7);
        }
    };
    auto waitfrag = [&](auto ST) {
      constexpr int st = decltype(ST)::value;
      wait14(tl[st][0], th[st][0]);
      wait14(tl[st][1], th[st][1]);
    };
    auto mma = [&](auto ST) {
      constexpr int st = decltype(ST)::value;
#pragma unroll
      for (int ks = 0; ks < 2; ++ks)
#pragma unroll
        for (int j = 0; j < 7; ++j) {
          bf16x8 bfr = __builtin_shufflevector(tl[st][ks][j], th[st][ks][j], 0, 1, 2, 3, 4, 5, 6, 7);
#pragma unroll
          for (int i = 0; i < 2; ++i)
            acc[i][j] = __builtin_amdgcn_mfma_f32_16x16x32_bf16(bfr, af[st][ks][i], acc[i][j], 0, 0, 0);
        }
    };
    auto step = [&](auto ST, int kt) {
      constexpr int st = decltype(ST)::value;
      wait_vm<0>();
      __builtin_amdgcn_s_barrier();
      if (kt + 2 < NKT) issue(kt + 2);
      rdfrag(std::integral_constant<int, st ^ 1>{});
      mma(ST);
      waitfrag(std::integral_constant<int, st ^ 1>{});
    };
    wait_vm<8>();
    __builtin_amdgcn_s_barrier();
    rdfrag(std::integral_constant<int, 0>{});
    waitfrag(std::integral_constant<int, 0>{});
#pragma unroll 1
    for (int kt = 0; kt < 6; kt += 2) {
      step(std::integral_constant<int, 0>{}, kt);
      step(std::integral_constant<int, 1>{}, kt + 1);
    }
    mma(std::integral_constant<int, 0>{});
    STAMPK(1, 4);
    u32x2 pk[2][7];
#pragma unroll
    for (int i = 0; i < 2; ++i) {
#pragma unroll
      for (int j = 0; j < 7; ++j) {
#pragma unroll
        for (int r = 0; r < 2; ++r) {
          unsigned p0 = pk2bf(acc[i][j][2 * r], acc[i][j][2 * r + 1]);
          unsigned p1 = pk2bf(lo_f(p0) + lo_f(bbp[j][r]), hi_f(p0) + hi_f(bbp[j][r]));
          float t0 = __builtin_fmaxf(lo_f(p1), 0.f), t1 = __builtin_fmaxf(hi_f(p1), 0.f);
          pk[i][j][r] = pk2bf(t0 * t0, t1 * t1);
        }
      }
    }
#pragma unroll
    for (int i = 0; i < 2; ++i) {
      u16* hrow = hbuf + (size_t)(m0 + w * 32 + i * 16 + fr) * HS + n0;
#pragma unroll
      for (int P = 0; P < 3; ++P)
        *reinterpret_cast<u32x4*>(hrow + P * 32 + (fq & 1) * 16 + (fq >> 1) * 8) = pair_cols(pk[i][2 * P], pk[i][2 * P + 1]);
      *reinterpret_cast<u32x2*>(hrow + 96 + fq * 4) = pk[i][6];
    }
    STAMPK(1, 5);
    return;
  }
  const int cb = BIDX - G1_BLOCKS;
  const int nch = (cb + 256 < DM) ? 2 : 1;
  char* XS = smem;
  char* KM = smem + CV_XBYTES;
  STAMPK(1, 0);
  u32x4 kv, cur[2], prv[2], xv;
  float dsk, kinv;
  auto prefetch = [&](int d) {
    const u16* xrow = xnT + (size_t)d * SL;
    kv = *reinterpret_cast<const u32x4*>(kexp + (size_t)d * SL + tid * 8);
#pragma unroll
    for (int rep = 0; rep < 2; ++rep) {
      int gg = tid + rep * 256 - 3;
      cur[rep] = (u32x4){0u, 0u, 0u, 0u}; prv[rep] = (u32x4){0u, 0u, 0u, 0u};
      if (rep == 0 || tid < 4) {
        if (gg >= 0 && gg < 256) cur[rep] = *reinterpret_cast<const u32x4*>(xrow + gg * 8);
        if (gg >= 1 && gg <= 256) prv[rep] = *reinterpret_cast<const u32x4*>(xrow + (gg - 1) * 8);
      }
    }
    xv = *reinterpret_cast<const u32x4*>(xrow + tid * 8);
    dsk = bf2f(dskip[d]);
    kinv = kinvp[d];
  };
  prefetch(cb);
  for (int i = tid; i < CV_KPAD * CV_KB / 16; i += 256)
    *reinterpret_cast<u32x4*>(KM + i * 16) = (u32x4){0u, 0u, 0u, 0u};
  for (int ch = 0; ch < nch; ++ch) {
    const int d = cb + ch * 256;
#pragma unroll
    for (int rep = 0; rep < 2; ++rep) {
      if (rep == 0 || tid < 4) {
        int gg = tid + rep * 256 - 3;
        int q = 256 - gg;
        unsigned Dw[8];
#pragma unroll
        for (int k = 0; k < 4; ++k) {
          Dw[k] = pk2h(lo_f(prv[rep][k]), hi_f(prv[rep][k]));
          Dw[4 + k] = pk2h(lo_f(cur[rep][k]), hi_f(cur[rep][k]));
        }
#pragma unroll
        for (int c = 0; c < 8; ++c) {
          u32x4 o;
#pragma unroll
          for (int k = 0; k < 4; ++k) {
            const int la = 8 + c - 2 * k;
            unsigned val;
            if (la & 1) {
              unsigned dd = Dw[(la - 1) >> 1];
              val = (dd >> 16) | (dd << 16);
            } else {
              val = (Dw[la >> 1] & 0xffffu) | (Dw[(la >> 1) - 1] & 0xffff0000u);
            }
            o[k] = val;
          }
          *reinterpret_cast<u32x4*>(XS + c * CV_XS + q * 16) = o;
        }
      }
    }
    {
      const int l0 = tid * 8;
      *reinterpret_cast<u32x4*>(KM + (CV_KPAD + (l0 >> 5)) * CV_KB + (l0 & 31) * 2) = kv;
    }
    const u32x4 xv_c = xv;
    const float dsk_c = dsk, kinv_c = kinv;
    bsync();
    if (ch == 0) STAMPK(1, 2);
    if (ch + 1 < nch) prefetch(d + 256);
    f32x16 acc0, acc1;
#pragma unroll
    for (int i = 0; i < 16; ++i) { acc0[i] = 0.f; acc1[i] = 0.f; }
    {
      const int t0 = lane & 31, hh = lane >> 5;
      const int c = t0 & 7;
      const char* abase = XS + c * CV_XS + 2 * (2048 - 8 * (t0 >> 3) + 8 * hh);
      const char* bbase = KM + (CV_KPAD + t0) * CV_KB + 16 * hh;
#pragma unroll 2
      for (int it = 0; it < 8; ++it) {
        const int dl = w + 4 * it;
#pragma unroll
        for (int ks = 0; ks < 2; ++ks) {
          f16x8 a0 = *reinterpret_cast<const f16x8*>(abase - 64 * dl + 32 * ks);
          f16x8 a1 = *reinterpret_cast<const f16x8*>(abase - 64 * (dl + 32) + 32 * ks);
          const char* b0p = bbase - dl * CV_KB + 32 * ks;
          f16x8 b0 = *reinterpret_cast<const f16x8*>(b0p);
          f16x8 b1v = *reinterpret_cast<const f16x8*>(b0p + 32 * CV_KB);
          acc0 = __builtin_amdgcn_mfma_f32_32x32x16_f16(a0, b0, acc0, 0, 0, 0);
          acc1 = __builtin_amdgcn_mfma_f32_32x32x16_f16(a0, b1v, acc1, 0, 0, 0);
          acc1 = __builtin_amdgcn_mfma_f32_32x32x16_f16(a1, b0, acc1, 0, 0, 0);
        }
      }
    }
    bsync();
    if (ch == 0) STAMPK(1, 3);
    float* red = reinterpret_cast<float*>(smem);
    {
      const int col = lane & 31, hh = lane >> 5;
#pragma unroll
      for (int g = 0; g < 4; ++g) {
        f32x4 v0 = (f32x4){acc0[4 * g], acc0[4 * g + 1], acc0[4 * g + 2], acc0[4 * g + 3]};
        f32x4 v1 = (f32x4){acc1[4 * g], acc1[4 * g + 1], acc1[4 * g + 2], acc1[4 * g + 3]};
        *reinterpret_cast<f32x4*>(red + w * 2304 + 36 * col + 8 * g + 4 * hh) = v0;
        *reinterpret_cast<f32x4*>(red + w * 2304 + 36 * (32 + col) + 8 * g + 4 * hh) = v1;
      }
    }
    bsync();
    {
      const int t = tid * 8;
      f32x4 y0 = (f32x4){0.f, 0.f, 0.f, 0.f}, y1 = (f32x4){0.f, 0.f, 0.f, 0.f};
#pragma unroll
      for (int ww = 0; ww < 4; ++ww) {
        y0 += *reinterpret_cast<const f32x4*>(red + ww * 2304 + 36 * (tid >> 2) + 8 * (tid & 3));
        y1 += *reinterpret_cast<const f32x4*>(red + ww * 2304 + 36 * (tid >> 2) + 8 * (tid & 3) + 4);
      }
      u32x4 o;
#pragma unroll
      for (int j = 0; j < 4; ++j) {
        float ya = (j < 2 ? y0[2 * j] : y1[2 * j - 4]) * kinv_c;
        float yb = (j < 2 ? y0[2 * j + 1] : y1[2 * j - 3]) * kinv_c;
        o[j] = pk2bf(rbf(ya) + rbf(lo_f(xv_c[j]) * dsk_c), rbf(yb) + rbf(hi_f(xv_c[j]) * dsk_c));
      }
      *reinterpret_cast<u32x4*>(sT + (size_t)d * SL + t) = o;
    }
    if (ch == 0) STAMPK(1, 4);
    if (ch + 1 < nch) bsync();
  }
  STAMPK(1, 5);
}

constexpr int G2_BM = 64, G2_BN = 64, G2_BK = 128;
constexpr int G2_BLOCKS = (SL / G2_BM) * (DM / G2_BN);
constexpr int G2_ABYTES = G2_BM * G2_BK * 2;
constexpr int G2_STAGE = 32768;
#define G2NS 4
constexpr int G2_NS = G2NS;
constexpr int K2_LDS = G2_NS * G2_STAGE;

__device__ __forceinline__ void wait8(bf16x4 (&a)[2][2][2]) {
  asm volatile("s_waitcnt lgkmcnt(0)"
               : "+v"(a[0][0][0]), "+v"(a[0][0][1]), "+v"(a[0][1][0]), "+v"(a[0][1][1]),
                 "+v"(a[1][0][0]), "+v"(a[1][0][1]), "+v"(a[1][1][0]), "+v"(a[1][1][1]));
}
__device__ __forceinline__ int g2_bswz(int r) { return ((r >> 1) & 3) << 1; }

__global__ __launch_bounds__(256, 1) NOPK_ATTR void k2_gemm2(
    const u16* __restrict__ hbuf, const u16* __restrict__ w2, const u16* __restrict__ b2,
    const u16* __restrict__ u, const u16* __restrict__ sT, u16* __restrict__ out) {
  extern __shared__ __attribute__((aligned(16))) char smem[];
  const int tid = TIDX, lane = tid & 63, w = tid >> 6;
  const int bid = BIDX;
  const int xcd = bid & 7, rest = bid >> 3;
  const int mt_i = xcd * 4 + (rest & 3);
  const int nt_i = rest >> 2;
  const int m0 = mt_i * G2_BM, n0 = nt_i * G2_BN;
  const int fr = lane & 15, fq = lane >> 4;
  const int wm = w & 1, wn = w >> 1;
  STAMPK(2, 0);
  const int ncol = wn * 32 + (fq & 1) * 16 + (fq >> 1) * 8;
  u32x4 e_u[2];
  u16 e_s[2][8];
#pragma unroll
  for (int i = 0; i < 2; ++i) {
    const int m = m0 + wm * 32 + i * 16 + fr, n = n0 + ncol;
    e_u[i] = *reinterpret_cast<const u32x4*>(u + (size_t)m * DM + n);
#pragma unroll
    for (int r = 0; r < 8; ++r) e_s[i][r] = sT[(size_t)(n + r) * SL + m];
  }
  float bb[2][4];
#pragma unroll
  for (int j = 0; j < 2; ++j) {
    u32x2 t = *reinterpret_cast<const u32x2*>(b2 + n0 + wn * 32 + j * 16 + fq * 4);
    bb[j][0] = lo_f(t[0]); bb[j][1] = hi_f(t[0]); bb[j][2] = lo_f(t[1]); bb[j][3] = hi_f(t[1]);
  }
  const u16* srcA[2];
  const u16* srcB[2];
#pragma unroll
  for (int i = 0; i < 2; ++i) {
    int p = (2 * w + i) * 64 + lane;
    int row = p >> 3, ph = p & 7;
    srcA[i] = hbuf + (size_t)(m0 + row) * HS + (ph ^ (row & 7)) * 8;
    srcB[i] = w2 + (size_t)row * DM + n0 + (ph ^ g2_bswz(row)) * 8;
  }
  auto issue = [&](int kt) {
    char* base = smem + (kt & (G2_NS - 1)) * G2_STAGE + (2 * w) * 1024 + lane * 16;
#pragma unroll
    for (int hf = 0; hf < 2; ++hf)
#pragma unroll
      for (int i = 0; i < 2; ++i) {
        __builtin_amdgcn_global_load_lds((const unsigned*)(srcA[i] + kt * G2_BK + hf * 64),
                                         (unsigned*)(base + hf * 8192 + i * 1024), 16, 0, 0);
        __builtin_amdgcn_global_load_lds((const unsigned*)(srcB[i] + (size_t)(kt * G2_BK + hf * 64) * DM),
                                         (unsigned*)(base + G2_ABYTES + hf * 8192 + i * 1024), 16, 0, 0);
      }
  };
  f32x4 acc[2][2];
#pragma unroll
  for (int i = 0; i < 2; ++i)
#pragma unroll
    for (int j = 0; j < 2; ++j) acc[i][j] = (f32x4){0.f, 0.f, 0.f, 0.f};
  constexpr int NKT = FF / G2_BK;
#pragma unroll
  for (int s0 = 0; s0 < G2_NS - 1; ++s0) issue(s0);
  const int q4 = (lane & 15) >> 2, p4 = lane & 3;
  bf16x4 t[2][2][2][2][2];
  bf16x4 xa[2][2][2][2][2];
  auto rdfrag = [&](auto ST, int kt) {
    constexpr int st = decltype(ST)::value;
    const unsigned abase = lds_addr(smem) + (kt & (G2_NS - 1)) * G2_STAGE;
    const unsigned bbase = abase + G2_ABYTES;
#pragma unroll
    for (int j = 0; j < 2; ++j)
#pragma unroll
      for (int hf = 0; hf < 2; ++hf) {
        const int r0 = fq * 4 + q4 + 16 * hf;
        const int ch = (wn * 2 + j) * 2 + (p4 >> 1);
        const unsigned a0 = bbase + r0 * 128 + ((ch ^ g2_bswz(r0)) << 4) + (p4 & 1) * 8;
        t[st][0][0][j][hf] = tr_read<0>(a0);
        t[st][0][1][j][hf] = tr_read<32 * 128>(a0);
        t[st][1][0][j][hf] = tr_read<64 * 128>(a0);
        t[st][1][1][j][hf] = tr_read<96 * 128>(a0);
      }
#pragma unroll
    for (int ks = 0; ks < 2; ++ks)
#pragma unroll
      for (int i = 0; i < 2; ++i)
#pragma unroll
        for (int hf = 0; hf < 2; ++hf) {
          int row = wm * 32 + i * 16 + fr;
          int c0 = ks * 4 + (fq >> 1) + 2 * hf;
          const unsigned a0 = abase + row * 128 + ((c0 ^ (row & 7)) << 4) + (fq & 1) * 8;
          xa[st][0][ks][i][hf] = ld_b64<0>(a0);
          xa[st][1][ks][i][hf] = ld_b64<8192>(a0);
        }
  };
  auto waitfrag = [&](auto ST) {
    constexpr int st = decltype(ST)::value;
    wait8(t[st][0]);
    wait8(t[st][1]);
    wait8(xa[st][0]);
    wait8(xa[st][1]);
  };
  auto mma = [&](auto ST) {
    constexpr int st = decltype(ST)::value;
#pragma unroll
    for (int hv = 0; hv < 2; ++hv)
#pragma unroll
      for (int ks = 0; ks < 2; ++ks)
#pragma unroll
        for (int j = 0; j < 2; ++j) {
          bf16x8 bfr = __builtin_shufflevector(t[st][hv][ks][j][0], t[st][hv][ks][j][1], 0, 1, 2, 3, 4, 5, 6, 7);
#pragma unroll
          for (int i = 0; i < 2; ++i)
            acc[i][j] = __builtin_amdgcn_mfma_f32_16x16x32_bf16(
                bfr, __builtin_shufflevector(xa[st][hv][ks][i][0], xa[st][hv][ks][i][1], 0, 1, 2, 3, 4, 5, 6, 7),
                acc[i][j], 0, 0, 0);
        }
  };
  auto step = [&](auto ST, int kt) {
    constexpr int st = decltype(ST)::value;
    if (kt + 2 < NKT) wait_vm<8>(); else wait_vm<0>();
    __builtin_amdgcn_s_barrier();
    if (kt + 3 < NKT) issue(kt + 3);
    rdfrag(std::integral_constant<int, st ^ 1>{}, kt + 1);
    mma(ST);
    waitfrag(std::integral_constant<int, st ^ 1>{});
  };
  wait_vm<16>();
  __builtin_amdgcn_s_barrier();
  rdfrag(std::integral_constant<int, 0>{}, 0);
  waitfrag(std::integral_constant<int, 0>{});
#pragma unroll 1
  for (int kt = 0; kt < 12; kt += 2) {
    step(std::integral_constant<int, 0>{}, kt);
    step(std::integral_constant<int, 1>{}, kt + 1);
  }
  step(std::integral_constant<int, 0>{}, 12);
  mma(std::integral_constant<int, 1>{});
  STAMPK(2, 5);
#pragma unroll
  for (int i = 0; i < 2; ++i) {
    const int m = m0 + wm * 32 + i * 16 + fr, n = n0 + ncol;
    u32x2 gp[2];
#pragma unroll
    for (int j = 0; j < 2; ++j) {
#pragma unroll
      for (int r = 0; r < 2; ++r) {
        unsigned p0 = pk2bf(acc[i][j][2 * r], acc[i][j][2 * r + 1]);
        unsigned p1 = pk2bf(lo_f(p0) + bb[j][2 * r], hi_f(p0) + bb[j][2 * r + 1]);
        float z0 = lo_f(p1), z1 = hi_f(p1);
        gp[j][r] = pk2bf(silu_f(z0), silu_f(z1));
      }
    }
    const u32x4 gv = pair_cols(gp[0], gp[1]);
    u32x4 o;
#pragma unroll
    for (int e = 0; e < 4; ++e)
      o[e] = pk2bf(lo_f(e_u[i][e]) + rbf(bf2f(e_s[i][2 * e]) * lo_f(gv[e])),
                   hi_f(e_u[i][e]) + rbf(bf2f(e_s[i][2 * e + 1]) * hi_f(gv[e])));
    *reinterpret_cast<u32x4*>(out + (size_t)m * DM + n) = o;
  }
  STAMPK(2, 6);
}

#if defined(__HIP_DEVICE_COMPILE__)
#pragma clang attribute pop
#endif
extern "C" void kernel_launch(void* const* d_in, const int* in_sizes, int n_in,
                              void* d_out, int out_size, void* d_ws, size_t ws_size,
                              hipStream_t stream) {
  const u16* u = (const u16*)d_in[0];
  const u16* logA = (const u16*)d_in[1];
  const u16* Aim = (const u16*)d_in[2];
  const u16* Cre = (const u16*)d_in[3];
  const u16* Cim = (const u16*)d_in[4];
  const u16* logdt = (const u16*)d_in[5];
  const u16* dskip = (const u16*)d_in[6];
  const u16* w1 = (const u16*)d_in[8];
  const u16* b1 = (const u16*)d_in[9];
  const u16* w2 = (const u16*)d_in[10];
  const u16* b2 = (const u16*)d_in[11];
  u16* out = (u16*)d_out;
  char* ws = (char*)d_ws;
  u16* xn = (u16*)(ws);
  u16* xnT = (u16*)(ws + (2u << 20));
  u16* sT = (u16*)(ws + (4u << 20));
  u16* kexp = (u16*)(ws + (6u << 20));
  float* kinvp = (float*)(ws + (7u << 20) + (900u << 10));
  u16* hbuf = (u16*)(ws + (8u << 20));

  hipFuncSetAttribute((const void*)k0_ln_kgen, hipFuncAttributeMaxDynamicSharedMemorySize, K0_LDS);
  hipFuncSetAttribute((const void*)k1_gemm1_conv, hipFuncAttributeMaxDynamicSharedMemorySize, K1_LDS);
  hipFuncSetAttribute((const void*)k2_gemm2, hipFuncAttributeMaxDynamicSharedMemorySize, K2_LDS);
  k0_ln_kgen<<<LN_BLOCKS + DM, 256, K0_LDS, stream>>>(u, logA, Aim, Cre, Cim, logdt, xn, xnT, kexp, kinvp);
  k1_gemm1_conv<<<G1_BLOCKS + 256, 256, K1_LDS, stream>>>(xn, xnT, kexp, kinvp, w1, b1, dskip, hbuf, sT);
  k2_gemm2<<<G2_BLOCKS, 256, K2_LDS, stream>>>(hbuf, w2, b2, u, sT, out);
}
```

```cpp
#include <hip/hip_runtime.h>
#include <hip/hip_bf16.h>
#include <stdint.h>
#include <type_traits>

#if defined(__HIP_DEVICE_COMPILE__)
#pragma clang attribute push(__attribute__((target("no-packed-fp32-ops"))), apply_to = function)
#endif
typedef unsigned short u16;
typedef __attribute__((ext_vector_type(8))) short bf16x8;
typedef __attribute__((ext_vector_type(4))) short bf16x4;
typedef __attribute__((ext_vector_type(8))) _Float16 f16x8;
typedef __attribute__((ext_vector_type(2))) _Float16 f16x2;
typedef __attribute__((ext_vector_type(2))) __bf16 bf16x2;
typedef __attribute__((ext_vector_type(2))) float f32x2;
typedef __attribute__((ext_vector_type(4))) float f32x4;
typedef __attribute__((ext_vector_type(16))) float f32x16;
typedef __attribute__((ext_vector_type(4))) unsigned int u32x4;
typedef __attribute__((ext_vector_type(2))) unsigned int u32x2;
#define LDS_AS __attribute__((address_space(3)))

#define TIDX ((int)__builtin_amdgcn_workitem_id_x())
#define BIDX ((int)__builtin_amdgcn_workgroup_id_x())
__device__ __forceinline__ float u2f(unsigned v) { return __builtin_bit_cast(float, v); }
__device__ __forceinline__ unsigned f2u(float v) { return __builtin_bit_cast(unsigned, v); }
__device__ __forceinline__ void bsync() {
  __builtin_amdgcn_fence(__ATOMIC_RELEASE, "workgroup");
  __builtin_amdgcn_s_barrier();
  __builtin_amdgcn_fence(__ATOMIC_ACQUIRE, "workgroup");
}
__device__ __forceinline__ float shfl_xor_f(float v, int m, int lane) {
  return u2f((unsigned)__builtin_amdgcn_ds_bpermute((lane ^ m) << 2, (int)f2u(v)));
}
__device__ __forceinline__ float exp_acc(float x) {
  const float L2E_HI = 1.44269502162933349609375f, L2E_LO = 1.92596299112661746e-8f;
  float t = x * L2E_HI;
  float e = __builtin_fmaf(x, L2E_HI, -t) + x * L2E_LO;
  float r = __builtin_amdgcn_exp2f(t);
  return __builtin_fmaf(r, e * 0.693147180559945309f, r);
}

__device__ __forceinline__ float silu_f(float z) {
  float dn = 1.0f + exp_acc(-z);
  float r = __builtin_amdgcn_rcpf(dn);
  float q = z * r;
  float e = __builtin_fmaf(-q, dn, z);
  float res = __builtin_fmaf(e, r, q);
  return (dn < 3.0e38f) ? res : -0.0f * 0.f + (z * 0.f);
}

constexpr int SL = 2048;
constexpr int DM = 448;
constexpr int NMODE = 256;
constexpr int FF = 1792;
#define HPAD 64
constexpr int HS = FF + HPAD;
constexpr int KTAB = 48 * 16;

__device__ __forceinline__ float bf2f(u16 v) { return u2f(((unsigned)v) << 16); }
__device__ __forceinline__ unsigned pk2bf(float a, float b) {
  f32x2 v = {a, b};
  bf16x2 r = __builtin_convertvector(v, bf16x2);
  return *reinterpret_cast<unsigned*>(&r);
}
__device__ __forceinline__ u16 f2bf(float f) { return (u16)(pk2bf(f, 0.f) & 0xffffu); }
__device__ __forceinline__ float rbf(float f) { return u2f(pk2bf(f, 0.f) << 16); }
__device__ __forceinline__ float lo_f(unsigned v) { return u2f(v << 16); }
__device__ __forceinline__ float hi_f(unsigned v) { return u2f(v & 0xffff0000u); }
__device__ __forceinline__ unsigned pk2h(float a, float b) {
  f32x2 v = {a, b};
  f16x2 r = __builtin_convertvector(v, f16x2);
  return *reinterpret_cast<unsigned*>(&r);
}

template <int I, int N> struct Unroll {
  template <class F> static __device__ __forceinline__ void run(F& f) {
    f(std::integral_constant<int, I>{});
    Unroll<I + 1, N>::run(f);
  }
};
template <int N> struct Unroll<N, N> {
  template <class F> static __device__ __forceinline__ void run(F&) {}
};
template <int N> __device__ __forceinline__ void wait_vm() {
  asm volatile("s_waitcnt vmcnt(%0)" ::"n"(N) : "memory");
}
__device__ __forceinline__ void wait_lgkm0() { asm volatile("s_waitcnt lgkmcnt(0)" ::: "memory"); }

template <int OFF> __device__ __forceinline__ bf16x4 tr_read(unsigned addr) {
  bf16x4 r;
  asm volatile("ds_read_b64_tr_b16 %0, %1 offset:%2" : "=v"(r) : "v"(addr), "n"(OFF));
  return r;
}
__device__ __forceinline__ void wait14(bf16x4 (&a)[7], bf16x4 (&b)[7]) {
  asm volatile("s_waitcnt lgkmcnt(0)"
               : "+v"(a[0]), "+v"(a[1]), "+v"(a[2]), "+v"(a[3]), "+v"(a[4]), "+v"(a[5]), "+v"(a[6]),
                 "+v"(b[0]), "+v"(b[1]), "+v"(b[2]), "+v"(b[3]), "+v"(b[4]), "+v"(b[5]), "+v"(b[6]));
}
template <int OFF> __device__ __forceinline__ bf16x4 ld_b64(unsigned addr) {
  bf16x4 r;
  asm volatile("ds_read_b64 %0, %1 offset:%2" : "=v"(r) : "v"(addr), "n"(OFF));
  return r;
}
__device__ __forceinline__ unsigned lds_addr(const void* p) { return (unsigned)(size_t)p; }

#define STAMPK(k, s)
__device__ __forceinline__ u32x4 pair_cols(u32x2 a, u32x2 b) {
  auto r0 = __builtin_amdgcn_permlane16_swap(a[0], b[0], false, false);
  auto r1 = __builtin_amdgcn_permlane16_swap(a[1], b[1], false, false);
  return (u32x4){r0[0], r1[0], r0[1], r1[1]};
}
struct cf { float r, i; };
__device__ __forceinline__ cf cmul(cf a, cf b) {
  cf o; o.r = a.r * b.r - a.i * b.i; o.i = a.r * b.i + a.i * b.r; return o;
}
__device__ __forceinline__ cf cexp_p(float ar, float ai, float p) {
  float m = __builtin_amdgcn_exp2f((ar * p) * 1.4426950408889634f);
  float rev = (ai * p) * 0.15915494309189535f;
  rev = __builtin_amdgcn_fractf(rev);
  cf o; o.r = m * __builtin_amdgcn_cosf(rev); o.i = m * __builtin_amdgcn_sinf(rev); return o;
}
template <int CTRL> __device__ __forceinline__ float dpp_add(float s) {
  int t = __builtin_amdgcn_update_dpp(0, (int)f2u(s), CTRL, 0xf, 0xf, true);
  return s + u2f((unsigned)t);
}
__device__ __forceinline__ float red16(float s) {
  s = dpp_add<0xB1>(s);
  s = dpp_add<0x4E>(s);
  s = dpp_add<0x141>(s);
  s = dpp_add<0x140>(s);
  return s;
}

constexpr int LN_ROWS = 16;
constexpr int LN_BLOCKS = SL / LN_ROWS;
constexpr int KG_RS = 136;
constexpr int K0_LDS = 4 * 2 * 16 * KG_RS * 2 + 64;

__device__ __forceinline__ int ktab_index(int l) {
  int pos, r, b;
  if (l < 256) { pos = l; r = ((pos >> 7) << 3) + (pos & 7); b = (pos >> 3) & 15; return r * 16 + b; }
  pos = (int)rbf((float)l);
  if (pos < 512) { int q = pos - 256; r = 16 + ((q >> 7) << 2) + ((q >> 1) & 3); b = (q >> 3) & 15; }
  else if (pos < 1024) { int q = pos - 512; r = 24 + ((q >> 7) << 1) + ((q >> 2) & 1); b = (q >> 3) & 15; }
  else if (pos < 2048) { int q = pos - 1024; r = 32 + (q >> 7); b = (q >> 3) & 15; }
  else { r = 40; b = 0; }
  return r * 16 + b;
}

#define NOPK_ATTR
__global__ __launch_bounds__(256) NOPK_ATTR void k0_ln_kgen(
    const u16* __restrict__ u, const u16* __restrict__ logA, const u16* __restrict__ Aim,
    const u16* __restrict__ Cre, const u16* __restrict__ Cim, const u16* __restrict__ logdt,
    u16* __restrict__ xn, u16* __restrict__ xnT, u16* __restrict__ kexp, float* __restrict__ kinvp,
    const u16* __restrict__ w1) {
  extern __shared__ __attribute__((aligned(16))) char smem[];
  const int tid = TIDX, lane = tid & 63, w = tid >> 6;
  if (BIDX < LN_BLOCKS) {
    u16* tile = reinterpret_cast<u16*>(smem);
    constexpr int TS = DM + 8;
    STAMPK(0, 0);
    const int rb = tid >> 4, l16 = tid & 15;
    const int row = BIDX * LN_ROWS + rb;
    u32x2 v[7];
#pragma unroll
    for (int i = 0; i < 7; ++i)
      v[i] = *reinterpret_cast<const u32x2*>(u + (size_t)row * DM + i * 64 + l16 * 4);
    float x[28];
#pragma unroll
    for (int i = 0; i < 7; ++i) {
      x[4 * i] = lo_f(v[i][0]); x[4 * i + 1] = hi_f(v[i][0]);
      x[4 * i + 2] = lo_f(v[i][1]); x[4 * i + 3] = hi_f(v[i][1]);
    }
    float s = 0.f;
#pragma unroll
    for (int j = 0; j < 28; ++j) s += x[j];
    s = red16(s);
    const float mean = s * (1.0f / DM);
    float q = 0.f;
#pragma unroll
    for (int j = 0; j < 28; ++j) { float dd = x[j] - mean; q += dd * dd; }
    q = red16(q);
    const float var_b = rbf(q * (1.0f / DM));
    const float mean_b = rbf(mean);
    const float den = rbf(__builtin_sqrtf(rbf(var_b + 1e-5f)));
#pragma unroll
    for (int i = 0; i < 7; ++i) {
      u32x2 o;
      o[0] = pk2bf(rbf(x[4 * i] - mean_b) / den, rbf(x[4 * i + 1] - mean_b) / den);
      o[1] = pk2bf(rbf(x[4 * i + 2] - mean_b) / den, rbf(x[4 * i + 3] - mean_b) / den);
      *reinterpret_cast<u32x2*>(xn + (size_t)row * DM + i * 64 + l16 * 4) = o;
      *reinterpret_cast<u32x2*>(tile + rb * TS + i * 64 + l16 * 4) = o;
    }
    bsync();
    STAMPK(0, 1);
    for (int d = tid; d < DM; d += 256) {
      unsigned pk[8];
#pragma unroll
      for (int r = 0; r < 8; ++r)
        pk[r] = (unsigned)tile[(2 * r) * TS + d] | ((unsigned)tile[(2 * r + 1) * TS + d] << 16);
      u32x4* dst = reinterpret_cast<u32x4*>(xnT + (size_t)d * SL + BIDX * LN_ROWS);
      dst[0] = (u32x4){pk[0], pk[1], pk[2], pk[3]};
      dst[1] = (u32x4){pk[4], pk[5], pk[6], pk[7]};
    }
    {
      unsigned wv[4];
      const int o = (BIDX >> 3) * 100352 + tid * 128;
#pragma unroll
      for (int q = 0; q < 4; ++q) {
        wv[q] = 0;
        if (q < 3 || tid < 16) wv[q] = *reinterpret_cast<const unsigned*>(reinterpret_cast<const char*>(w1) + o + q * 32768);
      }
      asm volatile("" ::"v"(wv[0]), "v"(wv[1]), "v"(wv[2]), "v"(wv[3]));
    }
    STAMPK(0, 5);
    return;
  }
  const int d = BIDX - LN_BLOCKS;
  u16* wbase = reinterpret_cast<u16*>(smem) + w * (2 * 16 * KG_RS);
  u16* Vb = wbase;
  u16* Wb = wbase + 16 * KG_RS;
  STAMPK(0, 0);
  const int idx = d * NMODE + tid;
  const u16 r_ldt = logdt[d], r_la = logA[idx], r_ai = Aim[idx], r_cr = Cre[idx], r_ci = Cim[idx];
  const float dt = rbf(exp_acc(bf2f(r_ldt)));
  const float Are = -rbf(exp_acc(bf2f(r_la)));
  const float ar = Are * dt;
  const float ai = bf2f(r_ai) * dt;
  cf C2; C2.r = 2.0f * (bf2f(r_cr) * dt); C2.i = 2.0f * (bf2f(r_ci) * dt);
  float* RM = reinterpret_cast<float*>(smem + 4 * 2 * 16 * KG_RS * 2);
  float wsc, wsc_inv;
  {
    float m = __builtin_fmaxf(__builtin_fabsf(C2.r), __builtin_fabsf(C2.i));
    float sm = __builtin_fabsf(C2.r) + __builtin_fabsf(C2.i);
#pragma unroll
    for (int o = 32; o > 0; o >>= 1) { m = __builtin_fmaxf(m, shfl_xor_f(m, o, lane)); sm += shfl_xor_f(sm, o, lane); }
    if (lane == 0) RM[w] = sm;
    int e = (int)((f2u(m) >> 23) & 0xff) - 127;
    if (m == 0.f || e > 100 || e < -100) e = 13;
    wsc = u2f((unsigned)(127 + 13 - e) << 23);
    wsc_inv = u2f((unsigned)(127 - 13 + e) << 23);
  }
  C2.r *= wsc; C2.i *= wsc;
  auto st = [&](u16* buf, int row, float re, float im) {
    *reinterpret_cast<unsigned*>(buf + row * KG_RS + 2 * lane) = pk2h(re, im);
  };
  {
    cf V1 = cexp_p(ar, ai, 8.f);
    cf V8 = cexp_p(ar, ai, 64.f);
    cf V[8];
    V[0].r = 1.f; V[0].i = 0.f;
    V[1] = V1;
    V[2] = cmul(V1, V1);
    V[3] = cmul(V[2], V1);
    V[4] = cmul(V[2], V[2]);
    V[5] = cmul(V[4], V1);
    V[6] = cmul(V[4], V[2]);
    V[7] = cmul(V[4], V[3]);
#pragma unroll
    for (int b = 0; b < 8; ++b) st(Vb, b, V[b].r, V[b].i);
    st(Vb, 8, V8.r, V8.i);
#pragma unroll
    for (int b = 1; b < 8; ++b) { cf t = cmul(V8, V[b]); st(Vb, 8 + b, t.r, t.i); }
  }
  cf U[8];
  {
    cf E1 = cexp_p(ar, ai, 1.f);
    cf E2 = cmul(E1, E1);
    cf E3 = cmul(E2, E1);
    cf E4 = cmul(E2, E2);
    U[0] = C2;
    U[1] = cmul(C2, E1);
    U[2] = cmul(C2, E2);
    U[3] = cmul(C2, E3);
    U[4] = cmul(C2, E4);
    U[5] = cmul(U[4], E1);
    U[6] = cmul(U[4], E2);
    U[7] = cmul(U[4], E3);
  }
  const cf F1 = cexp_p(ar, ai, 128.f);
  const cf F2 = cexp_p(ar, ai, 256.f);
  const cf F4 = cexp_p(ar, ai, 512.f);
  const cf F8 = cexp_p(ar, ai, 1024.f);
  const cf F3 = cmul(F2, F1);

  f32x4 acc[3];
#pragma unroll
  for (int mt = 0; mt < 3; ++mt) acc[mt] = (f32x4){0.f, 0.f, 0.f, 0.f};
  const int fr = lane & 15, fq = lane >> 4;
  asm volatile("" ::: "memory");
  f16x8 bv[4];
#pragma unroll
  for (int s4 = 0; s4 < 4; ++s4)
    bv[s4] = *reinterpret_cast<const f16x8*>(Vb + fr * KG_RS + 32 * s4 + 8 * fq);
  auto tile = [&](int mt, const cf* T) {
    asm volatile("" ::: "memory");
#pragma unroll
    for (int r = 0; r < 16; ++r) st(Wb, r, T[r].r, -T[r].i);
    asm volatile("" ::: "memory");
#pragma unroll
    for (int s4 = 0; s4 < 4; ++s4) {
      f16x8 a = *reinterpret_cast<const f16x8*>(Wb + fr * KG_RS + 32 * s4 + 8 * fq);
      acc[mt] = __builtin_amdgcn_mfma_f32_16x16x32_f16(a, bv[s4], acc[mt], 0, 0, 0);
    }
    asm volatile("" ::: "memory");
  };
  cf T[16];
#pragma unroll
  for (int e = 0; e < 8; ++e) { T[e] = U[e]; T[8 + e] = cmul(F1, U[e]); }
  STAMPK(0, 1);
  tile(0, T);
  STAMPK(0, 2);
  cf G[4];
  G[0] = F4; G[1] = cmul(F4, F1); G[2] = cmul(F4, F2); G[3] = cmul(F4, F3);
#pragma unroll
  for (int e = 0; e < 4; ++e) {
    T[e] = cmul(F2, U[2 * e]);
    T[4 + e] = cmul(F3, U[2 * e]);
    T[8 + 2 * e] = cmul(G[e], U[0]);
    T[8 + 2 * e + 1] = cmul(G[e], U[4]);
  }
  tile(1, T);
  {
    cf H0 = cmul(F8, U[0]);
    T[0] = H0; T[1] = cmul(H0, F1); T[2] = cmul(H0, F2); T[3] = cmul(H0, F3);
#pragma unroll
    for (int a2 = 0; a2 < 4; ++a2) T[4 + a2] = cmul(H0, G[a2]);
    T[8] = cmul(H0, F8);
#pragma unroll
    for (int r = 9; r < 16; ++r) { T[r].r = 0.f; T[r].i = 0.f; }
  }
  tile(2, T);
#pragma unroll
  for (int mt = 0; mt < 3; ++mt) acc[mt] *= wsc_inv;
  bsync();
  STAMPK(0, 3);
  float* red = reinterpret_cast<float*>(smem);
#pragma unroll
  for (int mt = 0; mt < 3; ++mt)
#pragma unroll
    for (int j = 0; j < 4; ++j) red[w * KTAB + (mt * 16 + fq * 4 + j) * 16 + fr] = acc[mt][j];
  bsync();
  float* KT = red + 4 * KTAB;
#pragma unroll
  for (int r = 0; r < 3; ++r) {
    int i = tid + r * 256;
    KT[i] = (red[i] + red[KTAB + i]) + (red[2 * KTAB + i] + red[3 * KTAB + i]);
  }
  bsync();
  {
    float m = (RM[0] + RM[1]) + (RM[2] + RM[3]);
    int e = (int)((f2u(m) >> 23) & 0xff) - 127;
    if (m == 0.f || e > 100 || e < -100) e = 14;
    const float kscale = u2f((unsigned)(127 + 14 - e) << 23);
    if (tid == 0) kinvp[d] = u2f((unsigned)(127 - 14 + e) << 23);
    const int l0 = tid * 8;
    u32x4 hv;
#pragma unroll
    for (int j = 0; j < 4; ++j) {
      float v0 = KT[ktab_index(l0 + 2 * j)], v1 = KT[ktab_index(l0 + 2 * j + 1)];
      hv[j] = pk2h(v0 * kscale, v1 * kscale);
    }
    *reinterpret_cast<u32x4*>(kexp + (size_t)d * SL + l0) = hv;
  }

  STAMPK(0, 5);
}

constexpr int G1_BM = 128, G1_BN = 112, G1_BK = 64;
constexpr int G1_BLOCKS = (SL / G1_BM) * (FF / G1_BN);
constexpr int G1_ABYTES = G1_BM * G1_BK * 2;
constexpr int G1_BRS = 224;
constexpr int G1_STAGE = 32768;
constexpr int G1_NS = 2;
constexpr int CV_XS = 4160;
constexpr int CV_XBYTES = 36864;
constexpr int CV_KB = 80;
constexpr int CV_KPAD = 31;
constexpr int CV_KBYTES = (CV_KPAD + 64) * CV_KB;
constexpr int K1_LDS = 81920;

__global__ __launch_bounds__(256, 2) NOPK_ATTR void k1_gemm1_conv(
    const u16* __restrict__ xn, const u16* __restrict__ xnT, const u16* __restrict__ kexp,
    const float* __restrict__ kinvp, const u16* __restrict__ w1, const u16* __restrict__ b1, const u16* __restrict__ dskip,
    u16* __restrict__ hbuf, u16* __restrict__ sT, const u16* __restrict__ w2) {
  extern __shared__ __attribute__((aligned(16))) char smem[];
  const int tid = TIDX, lane = tid & 63, w = tid >> 6;
  if (BIDX < G1_BLOCKS) {
    const int bid = BIDX;
    const int xcd = bid & 7, rest = bid >> 3;
    const int mt_i = xcd * 2 + (rest & 1);
    const int nt_i = rest >> 1;
    const int m0 = mt_i * G1_BM, n0 = nt_i * G1_BN;
    const int fr = lane & 15, fq = lane >> 4;
    STAMPK(1, 0);
    const int wu = __builtin_amdgcn_readfirstlane(w);
    const char* gbase = (wu < 2) ? reinterpret_cast<const char*>(xn) : reinterpret_cast<const char*>(w1);
    const unsigned kstepb = (wu < 2) ? (unsigned)(G1_BK * 2) : (unsigned)(G1_BK * FF * 2);
    unsigned off[8];
#pragma unroll
    for (int i = 0; i < 8; ++i) {
      int p = (8 * w + i) * 64 + lane;
      if (wu < 2) {
        int row = p >> 3, ph = p & 7;
        off[i] = (unsigned)(((m0 + row) * DM + (ph ^ (row & 7)) * 8) * 2);
      } else {
        int c = p - 1024; if (c >= 896) c -= 128;
        int kr = c / 14, ch = c - kr * 14;
        off[i] = (unsigned)((kr * FF + n0 + ch * 8) * 2);
      }
    }
    auto issue = [&](int kt) {
      char* base = smem + (kt & 1) * G1_STAGE + (8 * w) * 1024;
      const char* g = gbase + (size_t)kt * kstepb;
#pragma unroll
      for (int i = 0; i < 8; ++i)
        __builtin_amdgcn_global_load_lds((const unsigned*)(g + off[i]),
                                         (unsigned*)(base + i * 1024 + lane * 16), 16, 0, 0);
    };
    u32x2 bbp[7];
#pragma unroll
    for (int j = 0; j < 7; ++j) bbp[j] = *reinterpret_cast<const u32x2*>(b1 + n0 + j * 16 + fq * 4);
    f32x4 acc[2][7];
#pragma unroll
    for (int i = 0; i < 2; ++i)
#pragma unroll
      for (int j = 0; j < 7; ++j) acc[i][j] = (f32x4){0.f, 0.f, 0.f, 0.f};
    constexpr int NKT = DM / G1_BK;
    issue(0);
    issue(1);
    const int q4 = (lane & 15) >> 2, p4 = lane & 3;
    bf16x4 tl[2][2][7], th[2][2][7];
    bf16x8 af[2][2][2];
    auto rdfrag = [&](auto ST) {
      constexpr int st = decltype(ST)::value;
      const char* A = smem + st * G1_STAGE;
      const unsigned baddr = lds_addr(smem) + st * G1_STAGE + G1_ABYTES + (fq * 4 + q4) * G1_BRS + p4 * 8;
      auto rd = [&](auto J) {
        constexpr int j = decltype(J)::value;
        tl[st][0][j] = tr_read<j * 32>(baddr);
        th[st][0][j] = tr_read<j * 32 + 16 * G1_BRS>(baddr);
        tl[st][1][j] = tr_read<j * 32 + 32 * G1_BRS>(baddr);
        th[st][1][j] = tr_read<j * 32 + 48 * G1_BRS>(baddr);
      };
      Unroll<0, 7>::run(rd);
#pragma unroll
      for (int ks = 0; ks < 2; ++ks)
#pragma unroll
        for (int i = 0; i < 2; ++i) {
          int row = w * 32 + i * 16 + fr;
          int c0 = ks * 4 + (fq >> 1), c1 = c0 + 2;
          bf16x4 x0 = *reinterpret_cast<const bf16x4*>(A + row * 128 + ((c0 ^ (row & 7)) << 4) + (fq & 1) * 8);
          bf16x4 x1 = *reinterpret_cast<const bf16x4*>(A + row * 128 + ((c1 ^ (row & 7)) << 4) + (fq & 1) * 8);
          af[st][ks][i] = __builtin_shufflevector(x0, x1, 0, 1, 2, 3, 4, 5, 6, 7);
        }
    };
    auto waitfrag = [&](auto ST) {
      constexpr int st = decltype(ST)::value;
      wait14(tl[st][0], th[st][0]);
      wait14(tl[st][1], th[st][1]);
    };
    auto mma = [&](auto ST) {
      constexpr int st = decltype(ST)::value;
#pragma unroll
      for (int ks = 0; ks < 2; ++ks)
#pragma unroll
        for (int j = 0; j < 7; ++j) {
          bf16x8 bfr = __builtin_shufflevector(tl[st][ks][j], th[st][ks][j], 0, 1, 2, 3, 4, 5, 6, 7);
#pragma unroll
          for (int i = 0; i < 2; ++i)
            acc[i][j] = __builtin_amdgcn_mfma_f32_16x16x32_bf16(bfr, af[st][ks][i], acc[i][j], 0, 0, 0);
        }
    };
    auto step = [&](auto ST, int kt) {
      constexpr int st = decltype(ST)::value;
      wait_vm<0>();
      __builtin_amdgcn_s_barrier();
      if (kt + 2 < NKT) issue(kt + 2);
      rdfrag(std::integral_constant<int, st ^ 1>{});
      mma(ST);
      waitfrag(std::integral_constant<int, st ^ 1>{});
    };
    wait_vm<8>();
    __builtin_amdgcn_s_barrier();
    rdfrag(std::integral_constant<int, 0>{});
    waitfrag(std::integral_constant<int, 0>{});
#pragma unroll 1
    for (int kt = 0; kt < 6; kt += 2) {
      step(std::integral_constant<int, 0>{}, kt);
      step(std::integral_constant<int, 1>{}, kt + 1);
    }
    mma(std::integral_constant<int, 0>{});
    STAMPK(1, 4);
    u32x2 pk[2][7];
#pragma unroll
    for (int i = 0; i < 2; ++i) {
#pragma unroll
      for (int j = 0; j < 7; ++j) {
#pragma unroll
        for (int r = 0; r < 2; ++r) {
          unsigned p0 = pk2bf(acc[i][j][2 * r], acc[i][j][2 * r + 1]);
          unsigned p1 = pk2bf(lo_f(p0) + lo_f(bbp[j][r]), hi_f(p0) + hi_f(bbp[j][r]));
          float t0 = __builtin_fmaxf(lo_f(p1), 0.f), t1 = __builtin_fmaxf(hi_f(p1), 0.f);
          pk[i][j][r] = pk2bf(t0 * t0, t1 * t1);
        }
      }
    }
#pragma unroll
    for (int i = 0; i < 2; ++i) {
      u16* hrow = hbuf + (size_t)(m0 + w * 32 + i * 16 + fr) * HS + n0;
#pragma unroll
      for (int P = 0; P < 3; ++P)
        *reinterpret_cast<u32x4*>(hrow + P * 32 + (fq & 1) * 16 + (fq >> 1) * 8) = pair_cols(pk[i][2 * P], pk[i][2 * P + 1]);
      *reinterpret_cast<u32x2*>(hrow + 96 + fq * 4) = pk[i][6];
    }
    {
      unsigned warm0, warm1 = 0;
      const int o = (bid >> 3) * 50176 + tid * 128;
      warm0 = *reinterpret_cast<const unsigned*>(reinterpret_cast<const char*>(w2) + o);
      if (tid < 136) warm1 = *reinterpret_cast<const unsigned*>(reinterpret_cast<const char*>(w2) + o + 32768);
      asm volatile("" ::"v"(warm0), "v"(warm1));
    }
    STAMPK(1, 5);
    return;
  }
  const int cb = BIDX - G1_BLOCKS;
  const int nch = (cb + 256 < DM) ? 2 : 1;
  char* XS = smem;
  char* KM = smem + CV_XBYTES;
  STAMPK(1, 0);
  u32x4 kv, cur[2], prv[2], xv;
  float dsk, kinv;
  auto prefetch = [&](int d) {
    const u16* xrow = xnT + (size_t)d * SL;
    kv = *reinterpret_cast<const u32x4*>(kexp + (size_t)d * SL + tid * 8);
#pragma unroll
    for (int rep = 0; rep < 2; ++rep) {
      int gg = tid + rep * 256 - 3;
      cur[rep] = (u32x4){0u, 0u, 0u, 0u}; prv[rep] = (u32x4){0u, 0u, 0u, 0u};
      if (rep == 0 || tid < 4) {
        if (gg >= 0 && gg < 256) cur[rep] = *reinterpret_cast<const u32x4*>(xrow + gg * 8);
        if (gg >= 1 && gg <= 256) prv[rep] = *reinterpret_cast<const u32x4*>(xrow + (gg - 1) * 8);
      }
    }
    xv = *reinterpret_cast<const u32x4*>(xrow + tid * 8);
    dsk = bf2f(dskip[d]);
    kinv = kinvp[d];
  };
  prefetch(cb);
  for (int i = tid; i < CV_KPAD * CV_KB / 16; i += 256)
    *reinterpret_cast<u32x4*>(KM + i * 16) = (u32x4){0u, 0u, 0u, 0u};
  for (int ch = 0; ch < nch; ++ch) {
    const int d = cb + ch * 256;
#pragma unroll
    for (int rep = 0; rep < 2; ++rep) {
      if (rep == 0 || tid < 4) {
        int gg = tid + rep * 256 - 3;
        int q = 256 - gg;
        unsigned Dw[8];
#pragma unroll
        for (int k = 0; k < 4; ++k) {
          Dw[k] = pk2h(lo_f(prv[rep][k]), hi_f(prv[rep][k]));
          Dw[4 + k] = pk2h(lo_f(cur[rep][k]), hi_f(cur[rep][k]));
        }
#pragma unroll
        for (int c = 0; c < 8; ++c) {
          u32x4 o;
#pragma unroll
          for (int k = 0; k < 4; ++k) {
            const int la = 8 + c - 2 * k;
            unsigned val;
            if (la & 1) {
              unsigned dd = Dw[(la - 1) >> 1];
              val = (dd >> 16) | (dd << 16);
            } else {
              val = (Dw[la >> 1] & 0xffffu) | (Dw[(la >> 1) - 1] & 0xffff0000u);
            }
            o[k] = val;
          }
          *reinterpret_cast<u32x4*>(XS + c * CV_XS + q * 16) = o;
        }
      }
    }
    {
      const int l0 = tid * 8;
      *reinterpret_cast<u32x4*>(KM + (CV_KPAD + (l0 >> 5)) * CV_KB + (l0 & 31) * 2) = kv;
    }
    const u32x4 xv_c = xv;
    const float dsk_c = dsk, kinv_c = kinv;
    bsync();
    if (ch == 0) STAMPK(1, 2);
    if (ch + 1 < nch) prefetch(d + 256);
    f32x16 acc0, acc1;
#pragma unroll
    for (int i = 0; i < 16; ++i) { acc0[i] = 0.f; acc1[i] = 0.f; }
    {
      const int t0 = lane & 31, hh = lane >> 5;
      const int c = t0 & 7;
      const char* abase = XS + c * CV_XS + 2 * (2048 - 8 * (t0 >> 3) + 8 * hh);
      const char* bbase = KM + (CV_KPAD + t0) * CV_KB + 16 * hh;
#pragma unroll 2
      for (int it = 0; it < 8; ++it) {
        const int dl = w + 4 * it;
#pragma unroll
        for (int ks = 0; ks < 2; ++ks) {
          f16x8 a0 = *reinterpret_cast<const f16x8*>(abase - 64 * dl + 32 * ks);
          f16x8 a1 = *reinterpret_cast<const f16x8*>(abase - 64 * (dl + 32) + 32 * ks);
          const char* b0p = bbase - dl * CV_KB + 32 * ks;
          f16x8 b0 = *reinterpret_cast<const f16x8*>(b0p);
          f16x8 b1v = *reinterpret_cast<const f16x8*>(b0p + 32 * CV_KB);
          acc0 = __builtin_amdgcn_mfma_f32_32x32x16_f16(a0, b0, acc0, 0, 0, 0);
          acc1 = __builtin_amdgcn_mfma_f32_32x32x16_f16(a0, b1v, acc1, 0, 0, 0);
          acc1 = __builtin_amdgcn_mfma_f32_32x32x16_f16(a1, b0, acc1, 0, 0, 0);
        }
      }
    }
    bsync();
    if (ch == 0) STAMPK(1, 3);
    float* red = reinterpret_cast<float*>(smem);
    {
      const int col = lane & 31, hh = lane >> 5;
#pragma unroll
      for (int g = 0; g < 4; ++g) {
        f32x4 v0 = (f32x4){acc0[4 * g], acc0[4 * g + 1], acc0[4 * g + 2], acc0[4 * g + 3]};
        f32x4 v1 = (f32x4){acc1[4 * g], acc1[4 * g + 1], acc1[4 * g + 2], acc1[4 * g + 3]};
        *reinterpret_cast<f32x4*>(red + w * 2304 + 36 * col + 8 * g + 4 * hh) = v0;
        *reinterpret_cast<f32x4*>(red + w * 2304 + 36 * (32 + col) + 8 * g + 4 * hh) = v1;
      }
    }
    bsync();
    {
      const int t = tid * 8;
      f32x4 y0 = (f32x4){0.f, 0.f, 0.f, 0.f}, y1 = (f32x4){0.f, 0.f, 0.f, 0.f};
#pragma unroll
      for (int ww = 0; ww < 4; ++ww) {
        y0 += *reinterpret_cast<const f32x4*>(red + ww * 2304 + 36 * (tid >> 2) + 8 * (tid & 3));
        y1 += *reinterpret_cast<const f32x4*>(red + ww * 2304 + 36 * (tid >> 2) + 8 * (tid & 3) + 4);
      }
      u32x4 o;
#pragma unroll
      for (int j = 0; j < 4; ++j) {
        float ya = (j < 2 ? y0[2 * j] : y1[2 * j - 4]) * kinv_c;
        float yb = (j < 2 ? y0[2 * j + 1] : y1[2 * j - 3]) * kinv_c;
        o[j] = pk2bf(rbf(ya) + rbf(lo_f(xv_c[j]) * dsk_c), rbf(yb) + rbf(hi_f(xv_c[j]) * dsk_c));
      }
      *reinterpret_cast<u32x4*>(sT + (size_t)d * SL + t) = o;
    }
    if (ch == 0) STAMPK(1, 4);
    if (ch + 1 < nch) bsync();
  }

  STAMPK(1, 5);
}

constexpr int G2_BM = 64, G2_BN = 64, G2_BK = 128;
constexpr int G2_BLOCKS = (SL / G2_BM) * (DM / G2_BN);
constexpr int G2_ABYTES = G2_BM * G2_BK * 2;
constexpr int G2_STAGE = 32768;
#define G2NS 4
constexpr int G2_NS = G2NS;
constexpr int K2_LDS = G2_NS * G2_STAGE;

__device__ __forceinline__ void wait8(bf16x4 (&a)[2][2][2]) {
  asm volatile("s_waitcnt lgkmcnt(0)"
               : "+v"(a[0][0][0]), "+v"(a[0][0][1]), "+v"(a[0][1][0]), "+v"(a[0][1][1]),
                 "+v"(a[1][0][0]), "+v"(a[1][0][1]), "+v"(a[1][1][0]), "+v"(a[1][1][1]));
}
__device__ __forceinline__ int g2_bswz(int r) { return ((r >> 1) & 3) << 1; }

__global__ __launch_bounds__(256, 1) NOPK_ATTR void k2_gemm2(
    const u16* __restrict__ hbuf, const u16* __restrict__ w2, const u16* __restrict__ b2,
    const u16* __restrict__ u, const u16* __restrict__ sT, u16* __restrict__ out) {
  extern __shared__ __attribute__((aligned(16))) char smem[];
  const int tid = TIDX, lane = tid & 63, w = tid >> 6;
  const int bid = BIDX;
  const int xcd = bid & 7, rest = bid >> 3;
  const int mt_i = xcd * 4 + (rest & 3);
  const int nt_i = rest >> 2;
  const int m0 = mt_i * G2_BM, n0 = nt_i * G2_BN;
  const int fr = lane & 15, fq = lane >> 4;
  const int wm = w & 1, wn = w >> 1;
  STAMPK(2, 0);
  const int ncol = wn * 32 + (fq & 1) * 16 + (fq >> 1) * 8;
  u32x4 e_u[2];
  u16 e_s[2][8];
#pragma unroll
  for (int i = 0; i < 2; ++i) {
    const int m = m0 + wm * 32 + i * 16 + fr, n = n0 + ncol;
    e_u[i] = *reinterpret_cast<const u32x4*>(u + (size_t)m * DM + n);
#pragma unroll
    for (int r = 0; r < 8; ++r) e_s[i][r] = sT[(size_t)(n + r) * SL + m];
  }
  float bb[2][4];
#pragma unroll
  for (int j = 0; j < 2; ++j) {
    u32x2 t = *reinterpret_cast<const u32x2*>(b2 + n0 + wn * 32 + j * 16 + fq * 4);
    bb[j][0] = lo_f(t[0]); bb[j][1] = hi_f(t[0]); bb[j][2] = lo_f(t[1]); bb[j][3] = hi_f(t[1]);
  }
  const u16* srcA[2];
  const u16* srcB[2];
#pragma unroll
  for (int i = 0; i < 2; ++i) {
    int p = (2 * w + i) * 64 + lane;
    int row = p >> 3, ph = p & 7;
    srcA[i] = hbuf + (size_t)(m0 + row) * HS + (ph ^ (row & 7)) * 8;
    srcB[i] = w2 + (size_t)row * DM + n0 + (ph ^ g2_bswz(row)) * 8;
  }
  auto issue = [&](int kt) {
    char* base = smem + (kt % G2_NS) * G2_STAGE + (2 * w) * 1024 + lane * 16;
#pragma unroll
    for (int hf = 0; hf < 2; ++hf)
#pragma unroll
      for (int i = 0; i < 2; ++i) {
        __builtin_amdgcn_global_load_lds((const unsigned*)(srcA[i] + kt * G2_BK + hf * 64),
                                         (unsigned*)(base + hf * 8192 + i * 1024), 16, 0, 0);
        __builtin_amdgcn_global_load_lds((const unsigned*)(srcB[i] + (size_t)(kt * G2_BK + hf * 64) * DM),
                                         (unsigned*)(base + G2_ABYTES + hf * 8192 + i * 1024), 16, 0, 0);
      }
  };
  f32x4 acc[2][2];
#pragma unroll
  for (int i = 0; i < 2; ++i)
#pragma unroll
    for (int j = 0; j < 2; ++j) acc[i][j] = (f32x4){0.f, 0.f, 0.f, 0.f};
  constexpr int NKT = FF / G2_BK;
#pragma unroll
  for (int s0 = 0; s0 < G2_NS - 1; ++s0) issue(s0);
  const int q4 = (lane & 15) >> 2, p4 = lane & 3;
  bf16x4 t[2][2][2][2][2];
  bf16x4 xa[2][2][2][2][2];
  auto rdfrag = [&](auto KT) {
    constexpr int kt = decltype(KT)::value;
    constexpr int st = kt & 1;
    const char* A = smem + (kt % G2_NS) * G2_STAGE;
    const unsigned bbase = lds_addr(smem) + (kt % G2_NS) * G2_STAGE + G2_ABYTES;
#pragma unroll
    for (int j = 0; j < 2; ++j)
#pragma unroll
      for (int hf = 0; hf < 2; ++hf) {
        const int r0 = fq * 4 + q4 + 16 * hf;
        const int ch = (wn * 2 + j) * 2 + (p4 >> 1);
        const unsigned a0 = bbase + r0 * 128 + ((ch ^ g2_bswz(r0)) << 4) + (p4 & 1) * 8;
        t[st][0][0][j][hf] = tr_read<0>(a0);
        t[st][0][1][j][hf] = tr_read<32 * 128>(a0);
        t[st][1][0][j][hf] = tr_read<64 * 128>(a0);
        t[st][1][1][j][hf] = tr_read<96 * 128>(a0);
      }
#pragma unroll
    for (int ks = 0; ks < 2; ++ks)
#pragma unroll
      for (int i = 0; i < 2; ++i)
#pragma unroll
        for (int hf = 0; hf < 2; ++hf) {
          int row = wm * 32 + i * 16 + fr;
          int c0 = ks * 4 + (fq >> 1) + 2 * hf;
          const unsigned a0 = lds_addr(A) + row * 128 + ((c0 ^ (row & 7)) << 4) + (fq & 1) * 8;
          xa[st][0][ks][i][hf] = ld_b64<0>(a0);
          xa[st][1][ks][i][hf] = ld_b64<8192>(a0);
        }
  };
  auto waitfrag = [&](auto ST) {
    constexpr int st = decltype(ST)::value;
    wait8(t[st][0]);
    wait8(t[st][1]);
    wait8(xa[st][0]);
    wait8(xa[st][1]);
  };
  wait_vm<8 * (G2_NS - 2)>();
  __builtin_amdgcn_s_barrier();
  STAMPK(2, 1);
  rdfrag(std::integral_constant<int, 0>{});
  waitfrag(std::integral_constant<int, 0>{});
  auto step = [&](auto KT) {
    constexpr int kt = decltype(KT)::value;
    constexpr int st = kt & 1;
    if constexpr (kt + 1 < NKT) {
      constexpr int last = (kt + G2_NS - 2) < (NKT - 1) ? (kt + G2_NS - 2) : (NKT - 1);
      wait_vm<8 * (last - (kt + 1))>();
      __builtin_amdgcn_s_barrier();
      if constexpr (kt == 3) STAMPK(2, 2);
      if constexpr (kt == 7) STAMPK(2, 3);
      if constexpr (kt == 11) STAMPK(2, 4);
      if constexpr (kt + G2_NS - 1 < NKT) issue(kt + G2_NS - 1);
      rdfrag(std::integral_constant<int, kt + 1>{});
    }
#pragma unroll
    for (int hv = 0; hv < 2; ++hv)
#pragma unroll
      for (int ks = 0; ks < 2; ++ks)
#pragma unroll
        for (int j = 0; j < 2; ++j) {
          bf16x8 bfr = __builtin_shufflevector(t[st][hv][ks][j][0], t[st][hv][ks][j][1], 0, 1, 2, 3, 4, 5, 6, 7);
#pragma unroll
          for (int i = 0; i < 2; ++i)
            acc[i][j] = __builtin_amdgcn_mfma_f32_16x16x32_bf16(
                bfr, __builtin_shufflevector(xa[st][hv][ks][i][0], xa[st][hv][ks][i][1], 0, 1, 2, 3, 4, 5, 6, 7),
                acc[i][j], 0, 0, 0);
        }
    if constexpr (kt + 1 < NKT) waitfrag(std::integral_constant<int, st ^ 1>{});
  };
  Unroll<0, NKT>::run(step);
  STAMPK(2, 5);
#pragma unroll
  for (int i = 0; i < 2; ++i) {
    const int m = m0 + wm * 32 + i * 16 + fr, n = n0 + ncol;
    u32x2 gp[2];
#pragma unroll
    for (int j = 0; j < 2; ++j) {
#pragma unroll
      for (int r = 0; r < 2; ++r) {
        unsigned p0 = pk2bf(acc[i][j][2 * r], acc[i][j][2 * r + 1]);
        unsigned p1 = pk2bf(lo_f(p0) + bb[j][2 * r], hi_f(p0) + bb[j][2 * r + 1]);
        float z0 = lo_f(p1), z1 = hi_f(p1);
        gp[j][r] = pk2bf(silu_f(z0), silu_f(z1));
      }
    }
    const u32x4 gv = pair_cols(gp[0], gp[1]);
    u32x4 o;
#pragma unroll
    for (int e = 0; e < 4; ++e)
      o[e] = pk2bf(lo_f(e_u[i][e]) + rbf(bf2f(e_s[i][2 * e]) * lo_f(gv[e])),
                   hi_f(e_u[i][e]) + rbf(bf2f(e_s[i][2 * e + 1]) * hi_f(gv[e])));
    *reinterpret_cast<u32x4*>(out + (size_t)m * DM + n) = o;
  }
  STAMPK(2, 6);
}

#if defined(__HIP_DEVICE_COMPILE__)
#pragma clang attribute pop
#endif
extern "C" void kernel_launch(void* const* d_in, const int* in_sizes, int n_in,
                              void* d_out, int out_size, void* d_ws, size_t ws_size,
                              hipStream_t stream) {
  const u16* u = (const u16*)d_in[0];
  const u16* logA = (const u16*)d_in[1];
  const u16* Aim = (const u16*)d_in[2];
  const u16* Cre = (const u16*)d_in[3];
  const u16* Cim = (const u16*)d_in[4];
  const u16* logdt = (const u16*)d_in[5];
  const u16* dskip = (const u16*)d_in[6];
  const u16* w1 = (const u16*)d_in[8];
  const u16* b1 = (const u16*)d_in[9];
  const u16* w2 = (const u16*)d_in[10];
  const u16* b2 = (const u16*)d_in[11];
  u16* out = (u16*)d_out;
  char* ws = (char*)d_ws;
  u16* xn = (u16*)(ws);
  u16* xnT = (u16*)(ws + (2u << 20));
  u16* sT = (u16*)(ws + (4u << 20));
  u16* kexp = (u16*)(ws + (6u << 20));
  float* kinvp = (float*)(ws + (7u << 20) + (900u << 10));
  u16* hbuf = (u16*)(ws + (8u << 20));

  hipFuncSetAttribute((const void*)k0_ln_kgen, hipFuncAttributeMaxDynamicSharedMemorySize, K0_LDS);
  hipFuncSetAttribute((const void*)k1_gemm1_conv, hipFuncAttributeMaxDynamicSharedMemorySize, K1_LDS);
  hipFuncSetAttribute((const void*)k2_gemm2, hipFuncAttributeMaxDynamicSharedMemorySize, K2_LDS);
  k0_ln_kgen<<<LN_BLOCKS + DM, 256, K0_LDS, stream>>>(u, logA, Aim, Cre, Cim, logdt, xn, xnT, kexp, kinvp, w1);
  k1_gemm1_conv<<<G1_BLOCKS + 256, 256, K1_LDS, stream>>>(xn, xnT, kexp, kinvp, w1, b1, dskip, hbuf, sT, w2);
  k2_gemm2<<<G2_BLOCKS, 256, K2_LDS, stream>>>(hbuf, w2, b2, u, sT, out);
}
```

```cpp
#include <hip/hip_runtime.h>
#include <hip/hip_bf16.h>
#include <stdint.h>
#include <type_traits>

#if defined(__HIP_DEVICE_COMPILE__)
#pragma clang attribute push(__attribute__((target("no-packed-fp32-ops"))), apply_to = function)
#endif
typedef unsigned short u16;
typedef __attribute__((ext_vector_type(8))) short bf16x8;
typedef __attribute__((ext_vector_type(4))) short bf16x4;
typedef __attribute__((ext_vector_type(8))) _Float16 f16x8;
typedef __attribute__((ext_vector_type(2))) _Float16 f16x2;
typedef __attribute__((ext_vector_type(2))) __bf16 bf16x2;
typedef __attribute__((ext_vector_type(2))) float f32x2;
typedef __attribute__((ext_vector_type(4))) float f32x4;
typedef __attribute__((ext_vector_type(16))) float f32x16;
typedef __attribute__((ext_vector_type(4))) unsigned int u32x4;
typedef __attribute__((ext_vector_type(2))) unsigned int u32x2;
#define LDS_AS __attribute__((address_space(3)))

#define TIDX ((int)__builtin_amdgcn_workitem_id_x())
#define BIDX ((int)__builtin_amdgcn_workgroup_id_x())
__device__ __forceinline__ float u2f(unsigned v) { return __builtin_bit_cast(float, v); }
__device__ __forceinline__ unsigned f2u(float v) { return __builtin_bit_cast(unsigned, v); }
__device__ __forceinline__ void bsync() {
  __builtin_amdgcn_fence(__ATOMIC_RELEASE, "workgroup");
  __builtin_amdgcn_s_barrier();
  __builtin_amdgcn_fence(__ATOMIC_ACQUIRE, "workgroup");
}
__device__ __forceinline__ float shfl_xor_f(float v, int m, int lane) {
  return u2f((unsigned)__builtin_amdgcn_ds_bpermute((lane ^ m) << 2, (int)f2u(v)));
}
__device__ __forceinline__ float exp_acc(float x) {
  const float L2E_HI = 1.44269502162933349609375f, L2E_LO = 1.92596299112661746e-8f;
  float t = x * L2E_HI;
  float e = __builtin_fmaf(x, L2E_HI, -t) + x * L2E_LO;
  float r = __builtin_amdgcn_exp2f(t);
  return __builtin_fmaf(r, e * 0.693147180559945309f, r);
}

__device__ __forceinline__ float silu_f(float z) {
  float dn = 1.0f + exp_acc(-z);
  float r = __builtin_amdgcn_rcpf(dn);
  float q = z * r;
  float e = __builtin_fmaf(-q, dn, z);
  float res = __builtin_fmaf(e, r, q);
  return (dn < 3.0e38f) ? res : -0.0f * 0.f + (z * 0.f);
}

constexpr int SL = 2048;
constexpr int DM = 448;
constexpr int NMODE = 256;
constexpr int FF = 1792;
#define HPAD 64
constexpr int HS = FF + HPAD;
constexpr int KTAB = 48 * 16;

__device__ __forceinline__ float bf2f(u16 v) { return u2f(((unsigned)v) << 16); }
__device__ __forceinline__ unsigned pk2bf(float a, float b) {
  f32x2 v = {a, b};
  bf16x2 r = __builtin_convertvector(v, bf16x2);
  return *reinterpret_cast<unsigned*>(&r);
}
__device__ __forceinline__ u16 f2bf(float f) { return (u16)(pk2bf(f, 0.f) & 0xffffu); }
__device__ __forceinline__ float rbf(float f) { return u2f(pk2bf(f, 0.f) << 16); }
__device__ __forceinline__ float lo_f(unsigned v) { return u2f(v << 16); }
__device__ __forceinline__ float hi_f(unsigned v) { return u2f(v & 0xffff0000u); }
__device__ __forceinline__ unsigned pk2h(float a, float b) {
  f32x2 v = {a, b};
  f16x2 r = __builtin_convertvector(v, f16x2);
  return *reinterpret_cast<unsigned*>(&r);
}

template <int I, int N> struct Unroll {
  template <class F> static __device__ __forceinline__ void run(F& f) {
    f(std::integral_constant<int, I>{});
    Unroll<I + 1, N>::run(f);
  }
};
template <int N> struct Unroll<N, N> {
  template <class F> static __device__ __forceinline__ void run(F&) {}
};
template <int N> __device__ __forceinline__ void wait_vm() {
  asm volatile("s_waitcnt vmcnt(%0)" ::"n"(N) : "memory");
}
__device__ __forceinline__ void wait_lgkm0() { asm volatile("s_waitcnt lgkmcnt(0)" ::: "memory"); }

template <int OFF> __device__ __forceinline__ bf16x4 tr_read(unsigned addr) {
  bf16x4 r;
  asm volatile("ds_read_b64_tr_b16 %0, %1 offset:%2" : "=v"(r) : "v"(addr), "n"(OFF));
  return r;
}
__device__ __forceinline__ void wait14(bf16x4 (&a)[7], bf16x4 (&b)[7]) {
  asm volatile("s_waitcnt lgkmcnt(0)"
               : "+v"(a[0]), "+v"(a[1]), "+v"(a[2]), "+v"(a[3]), "+v"(a[4]), "+v"(a[5]), "+v"(a[6]),
                 "+v"(b[0]), "+v"(b[1]), "+v"(b[2]), "+v"(b[3]), "+v"(b[4]), "+v"(b[5]), "+v"(b[6]));
}
template <int OFF> __device__ __forceinline__ bf16x4 ld_b64(unsigned addr) {
  bf16x4 r;
  asm volatile("ds_read_b64 %0, %1 offset:%2" : "=v"(r) : "v"(addr), "n"(OFF));
  return r;
}
__device__ __forceinline__ unsigned lds_addr(const void* p) { return (unsigned)(size_t)p; }

#define STAMPK(k, s)
__device__ __forceinline__ u32x4 pair_cols(u32x2 a, u32x2 b) {
  auto r0 = __builtin_amdgcn_permlane16_swap(a[0], b[0], false, false);
  auto r1 = __builtin_amdgcn_permlane16_swap(a[1], b[1], false, false);
  return (u32x4){r0[0], r1[0], r0[1], r1[1]};
}
struct cf { float r, i; };
__device__ __forceinline__ cf cmul(cf a, cf b) {
  cf o; o.r = a.r * b.r - a.i * b.i; o.i = a.r * b.i + a.i * b.r; return o;
}
__device__ __forceinline__ cf cexp_p(float ar, float ai, float p) {
  float m = __builtin_amdgcn_exp2f((ar * p) * 1.4426950408889634f);
  float rev = (ai * p) * 0.15915494309189535f;
  rev = __builtin_amdgcn_fractf(rev);
  cf o; o.r = m * __builtin_amdgcn_cosf(rev); o.i = m * __builtin_amdgcn_sinf(rev); return o;
}
template <int CTRL> __device__ __forceinline__ float dpp_add(float s) {
  int t = __builtin_amdgcn_update_dpp(0, (int)f2u(s), CTRL, 0xf, 0xf, true);
  return s + u2f((unsigned)t);
}
__device__ __forceinline__ float red16(float s) {
  s = dpp_add<0xB1>(s);
  s = dpp_add<0x4E>(s);
  s = dpp_add<0x141>(s);
  s = dpp_add<0x140>(s);
  return s;
}

constexpr int LN_ROWS = 16;
constexpr int LN_BLOCKS = SL / LN_ROWS;
constexpr int KG_RS = 136;
constexpr int K0_LDS = 4 * 2 * 16 * KG_RS * 2 + 64;

__device__ __forceinline__ int ktab_index(int l) {
  int pos, r, b;
  if (l < 256) { pos = l; r = ((pos >> 7) << 3) + (pos & 7); b = (pos >> 3) & 15; return r * 16 + b; }
  pos = (int)rbf((float)l);
  if (pos < 512) { int q = pos - 256; r = 16 + ((q >> 7) << 2) + ((q >> 1) & 3); b = (q >> 3) & 15; }
  else if (pos < 1024) { int q = pos - 512; r = 24 + ((q >> 7) << 1) + ((q >> 2) & 1); b = (q >> 3) & 15; }
  else if (pos < 2048) { int q = pos - 1024; r = 32 + (q >> 7); b = (q >> 3) & 15; }
  else { r = 40; b = 0; }
  return r * 16 + b;
}

#define NOPK_ATTR
__global__ __launch_bounds__(256) NOPK_ATTR void k0_ln_kgen(
    const u16* __restrict__ u, const u16* __restrict__ logA, const u16* __restrict__ Aim,
    const u16* __restrict__ Cre, const u16* __restrict__ Cim, const u16* __restrict__ logdt,
    u16* __restrict__ xn, u16* __restrict__ xnT, u16* __restrict__ kexp, float* __restrict__ kinvp,
    const u16* __restrict__ w1) {
  extern __shared__ __attribute__((aligned(16))) char smem[];
  const int tid = TIDX, lane = tid & 63, w = tid >> 6;
  if (BIDX < LN_BLOCKS) {
    u16* tile = reinterpret_cast<u16*>(smem);
    constexpr int TS = DM + 8;
    STAMPK(0, 0);
    const int rb = tid >> 4, l16 = tid & 15;
    const int rblk = ((BIDX & 7) << 4) + (BIDX >> 3);
    const int row = rblk * LN_ROWS + rb;
    u32x2 v[7];
#pragma unroll
    for (int i = 0; i < 7; ++i)
      v[i] = *reinterpret_cast<const u32x2*>(u + (size_t)row * DM + i * 64 + l16 * 4);
    float x[28];
#pragma unroll
    for (int i = 0; i < 7; ++i) {
      x[4 * i] = lo_f(v[i][0]); x[4 * i + 1] = hi_f(v[i][0]);
      x[4 * i + 2] = lo_f(v[i][1]); x[4 * i + 3] = hi_f(v[i][1]);
    }
    float s = 0.f;
#pragma unroll
    for (int j = 0; j < 28; ++j) s += x[j];
    s = red16(s);
    const float mean = s * (1.0f / DM);
    float q = 0.f;
#pragma unroll
    for (int j = 0; j < 28; ++j) { float dd = x[j] - mean; q += dd * dd; }
    q = red16(q);
    const float var_b = rbf(q * (1.0f / DM));
    const float mean_b = rbf(mean);
    const float den = rbf(__builtin_sqrtf(rbf(var_b + 1e-5f)));
#pragma unroll
    for (int i = 0; i < 7; ++i) {
      u32x2 o;
      o[0] = pk2bf(rbf(x[4 * i] - mean_b) / den, rbf(x[4 * i + 1] - mean_b) / den);
      o[1] = pk2bf(rbf(x[4 * i + 2] - mean_b) / den, rbf(x[4 * i + 3] - mean_b) / den);
      *reinterpret_cast<u32x2*>(xn + (size_t)row * DM + i * 64 + l16 * 4) = o;
      *reinterpret_cast<u32x2*>(tile + rb * TS + i * 64 + l16 * 4) = o;
    }
    bsync();
    STAMPK(0, 1);
    for (int d = tid; d < DM; d += 256) {
      unsigned pk[8];
#pragma unroll
      for (int r = 0; r < 8; ++r)
        pk[r] = (unsigned)tile[(2 * r) * TS + d] | ((unsigned)tile[(2 * r + 1) * TS + d] << 16);
      u32x4* dst = reinterpret_cast<u32x4*>(xnT + (size_t)d * SL + rblk * LN_ROWS);
      dst[0] = (u32x4){pk[0], pk[1], pk[2], pk[3]};
      dst[1] = (u32x4){pk[4], pk[5], pk[6], pk[7]};
    }
    {
      unsigned wv[4];
      const int o = (BIDX >> 3) * 100352 + tid * 128;
#pragma unroll
      for (int q = 0; q < 4; ++q) {
        wv[q] = 0;
        if (q < 3 || tid < 16) wv[q] = *reinterpret_cast<const unsigned*>(reinterpret_cast<const char*>(w1) + o + q * 32768);
      }
      asm volatile("" ::"v"(wv[0]), "v"(wv[1]), "v"(wv[2]), "v"(wv[3]));
    }
    STAMPK(0, 5);
    return;
  }
  const int d = BIDX - LN_BLOCKS;
  u16* wbase = reinterpret_cast<u16*>(smem) + w * (2 * 16 * KG_RS);
  u16* Vb = wbase;
  u16* Wb = wbase + 16 * KG_RS;
  STAMPK(0, 0);
  const int idx = d * NMODE + tid;
  const u16 r_ldt = logdt[d], r_la = logA[idx], r_ai = Aim[idx], r_cr = Cre[idx], r_ci = Cim[idx];
  const float dt = rbf(exp_acc(bf2f(r_ldt)));
  const float Are = -rbf(exp_acc(bf2f(r_la)));
  const float ar = Are * dt;
  const float ai = bf2f(r_ai) * dt;
  cf C2; C2.r = 2.0f * (bf2f(r_cr) * dt); C2.i = 2.0f * (bf2f(r_ci) * dt);
  float* RM = reinterpret_cast<float*>(smem + 4 * 2 * 16 * KG_RS * 2);
  float wsc, wsc_inv;
  {
    float m = __builtin_fmaxf(__builtin_fabsf(C2.r), __builtin_fabsf(C2.i));
    float sm = __builtin_fabsf(C2.r) + __builtin_fabsf(C2.i);
#pragma unroll
    for (int o = 32; o > 0; o >>= 1) { m = __builtin_fmaxf(m, shfl_xor_f(m, o, lane)); sm += shfl_xor_f(sm, o, lane); }
    if (lane == 0) RM[w] = sm;
    int e = (int)((f2u(m) >> 23) & 0xff) - 127;
    if (m == 0.f || e > 100 || e < -100) e = 13;
    wsc = u2f((unsigned)(127 + 13 - e) << 23);
    wsc_inv = u2f((unsigned)(127 - 13 + e) << 23);
  }
  C2.r *= wsc; C2.i *= wsc;
  auto st = [&](u16* buf, int row, float re, float im) {
    *reinterpret_cast<unsigned*>(buf + row * KG_RS + 2 * lane) = pk2h(re, im);
  };
  {
    cf V1 = cexp_p(ar, ai, 8.f);
    cf V8 = cexp_p(ar, ai, 64.f);
    cf V[8];
    V[0].r = 1.f; V[0].i = 0.f;
    V[1] = V1;
    V[2] = cmul(V1, V1);
    V[3] = cmul(V[2], V1);
    V[4] = cmul(V[2], V[2]);
    V[5] = cmul(V[4], V1);
    V[6] = cmul(V[4], V[2]);
    V[7] = cmul(V[4], V[3]);
#pragma unroll
    for (int b = 0; b < 8; ++b) st(Vb, b, V[b].r, V[b].i);
    st(Vb, 8, V8.r, V8.i);
#pragma unroll
    for (int b = 1; b < 8; ++b) { cf t = cmul(V8, V[b]); st(Vb, 8 + b, t.r, t.i); }
  }
  cf U[8];
  {
    cf E1 = cexp_p(ar, ai, 1.f);
    cf E2 = cmul(E1, E1);
    cf E3 = cmul(E2, E1);
    cf E4 = cmul(E2, E2);
    U[0] = C2;
    U[1] = cmul(C2, E1);
    U[2] = cmul(C2, E2);
    U[3] = cmul(C2, E3);
    U[4] = cmul(C2, E4);
    U[5] = cmul(U[4], E1);
    U[6] = cmul(U[4], E2);
    U[7] = cmul(U[4], E3);
  }
  const cf F1 = cexp_p(ar, ai, 128.f);
  const cf F2 = cexp_p(ar, ai, 256.f);
  const cf F4 = cexp_p(ar, ai, 512.f);
  const cf F8 = cexp_p(ar, ai, 1024.f);
  const cf F3 = cmul(F2, F1);

  f32x4 acc[3];
#pragma unroll
  for (int mt = 0; mt < 3; ++mt) acc[mt] = (f32x4){0.f, 0.f, 0.f, 0.f};
  const int fr = lane & 15, fq = lane >> 4;
  asm volatile("" ::: "memory");
  f16x8 bv[4];
#pragma unroll
  for (int s4 = 0; s4 < 4; ++s4)
    bv[s4] = *reinterpret_cast<const f16x8*>(Vb + fr * KG_RS + 32 * s4 + 8 * fq);
  auto tile = [&](int mt, const cf* T) {
    asm volatile("" ::: "memory");
#pragma unroll
    for (int r = 0; r < 16; ++r) st(Wb, r, T[r].r, -T[r].i);
    asm volatile("" ::: "memory");
#pragma unroll
    for (int s4 = 0; s4 < 4; ++s4) {
      f16x8 a = *reinterpret_cast<const f16x8*>(Wb + fr * KG_RS + 32 * s4 + 8 * fq);
      acc[mt] = __builtin_amdgcn_mfma_f32_16x16x32_f16(a, bv[s4], acc[mt], 0, 0, 0);
    }
    asm volatile("" ::: "memory");
  };
  cf T[16];
#pragma unroll
  for (int e = 0; e < 8; ++e) { T[e] = U[e]; T[8 + e] = cmul(F1, U[e]); }
  STAMPK(0, 1);
  tile(0, T);
  STAMPK(0, 2);
  cf G[4];
  G[0] = F4; G[1] = cmul(F4, F1); G[2] = cmul(F4, F2); G[3] = cmul(F4, F3);
#pragma unroll
  for (int e = 0; e < 4; ++e) {
    T[e] = cmul(F2, U[2 * e]);
    T[4 + e] = cmul(F3, U[2 * e]);
    T[8 + 2 * e] = cmul(G[e], U[0]);
    T[8 + 2 * e + 1] = cmul(G[e], U[4]);
  }
  tile(1, T);
  {
    cf H0 = cmul(F8, U[0]);
    T[0] = H0; T[1] = cmul(H0, F1); T[2] = cmul(H0, F2); T[3] = cmul(H0, F3);
#pragma unroll
    for (int a2 = 0; a2 < 4; ++a2) T[4 + a2] = cmul(H0, G[a2]);
    T[8] = cmul(H0, F8);
#pragma unroll
    for (int r = 9; r < 16; ++r) { T[r].r = 0.f; T[r].i = 0.f; }
  }
  tile(2, T);
#pragma unroll
  for (int mt = 0; mt < 3; ++mt) acc[mt] *= wsc_inv;
  bsync();
  STAMPK(0, 3);
  float* red = reinterpret_cast<float*>(smem);
#pragma unroll
  for (int mt = 0; mt < 3; ++mt)
#pragma unroll
    for (int j = 0; j < 4; ++j) red[w * KTAB + (mt * 16 + fq * 4 + j) * 16 + fr] = acc[mt][j];
  bsync();
  float* KT = red + 4 * KTAB;
#pragma unroll
  for (int r = 0; r < 3; ++r) {
    int i = tid + r * 256;
    KT[i] = (red[i] + red[KTAB + i]) + (red[2 * KTAB + i] + red[3 * KTAB + i]);
  }
  bsync();
  {
    float m = (RM[0] + RM[1]) + (RM[2] + RM[3]);
    int e = (int)((f2u(m) >> 23) & 0xff) - 127;
    if (m == 0.f || e > 100 || e < -100) e = 14;
    const float kscale = u2f((unsigned)(127 + 14 - e) << 23);
    if (tid == 0) kinvp[d] = u2f((unsigned)(127 - 14 + e) << 23);
    const int l0 = tid * 8;
    u32x4 hv;
#pragma unroll
    for (int j = 0; j < 4; ++j) {
      float v0 = KT[ktab_index(l0 + 2 * j)], v1 = KT[ktab_index(l0 + 2 * j + 1)];
      hv[j] = pk2h(v0 * kscale, v1 * kscale);
    }
    *reinterpret_cast<u32x4*>(kexp + (size_t)d * SL + l0) = hv;
  }

  STAMPK(0, 5);
}

constexpr int G1_BM = 128, G1_BN = 112, G1_BK = 64;
constexpr int G1_BLOCKS = (SL / G1_BM) * (FF / G1_BN);
constexpr int G1_ABYTES = G1_BM * G1_BK * 2;
constexpr int G1_BRS = 224;
constexpr int G1_STAGE = 32768;
constexpr int G1_NS = 2;
constexpr int CV_XS = 4160;
constexpr int CV_XBYTES = 36864;
constexpr int CV_KB = 80;
constexpr int CV_KPAD = 31;
constexpr int CV_KBYTES = (CV_KPAD + 64) * CV_KB;
constexpr int K1_LDS = 81920;

__global__ __launch_bounds__(256, 2) NOPK_ATTR void k1_gemm1_conv(
    const u16* __restrict__ xn, const u16* __restrict__ xnT, const u16* __restrict__ kexp,
    const float* __restrict__ kinvp, const u16* __restrict__ w1, const u16* __restrict__ b1, const u16* __restrict__ dskip,
    u16* __restrict__ hbuf, u16* __restrict__ sT, const u16* __restrict__ w2) {
  extern __shared__ __attribute__((aligned(16))) char smem[];
  const int tid = TIDX, lane = tid & 63, w = tid >> 6;
  if (BIDX < G1_BLOCKS) {
    const int bid = BIDX;
    const int xcd = bid & 7, rest = bid >> 3;
    const int mt_i = xcd * 2 + (rest & 1);
    const int nt_i = rest >> 1;
    const int m0 = mt_i * G1_BM, n0 = nt_i * G1_BN;
    const int fr = lane & 15, fq = lane >> 4;
    STAMPK(1, 0);
    const int wu = __builtin_amdgcn_readfirstlane(w);
    const char* gbase = (wu < 2) ? reinterpret_cast<const char*>(xn) : reinterpret_cast<const char*>(w1);
    const unsigned kstepb = (wu < 2) ? (unsigned)(G1_BK * 2) : (unsigned)(G1_BK * FF * 2);
    unsigned off[8];
#pragma unroll
    for (int i = 0; i < 8; ++i) {
      int p = (8 * w + i) * 64 + lane;
      if (wu < 2) {
        int row = p >> 3, ph = p & 7;
        off[i] = (unsigned)(((m0 + row) * DM + (ph ^ (row & 7)) * 8) * 2);
      } else {
        int c = p - 1024; if (c >= 896) c -= 128;
        int kr = c / 14, ch = c - kr * 14;
        off[i] = (unsigned)((kr * FF + n0 + ch * 8) * 2);
      }
    }
    auto issue = [&](int kt) {
      char* base = smem + (kt & 1) * G1_STAGE + (8 * w) * 1024;
      const char* g = gbase + (size_t)kt * kstepb;
#pragma unroll
      for (int i = 0; i < 8; ++i)
        __builtin_amdgcn_global_load_lds((const unsigned*)(g + off[i]),
                                         (unsigned*)(base + i * 1024 + lane * 16), 16, 0, 0);
    };
    u32x2 bbp[7];
#pragma unroll
    for (int j = 0; j < 7; ++j) bbp[j] = *reinterpret_cast<const u32x2*>(b1 + n0 + j * 16 + fq * 4);
    f32x4 acc[2][7];
#pragma unroll
    for (int i = 0; i < 2; ++i)
#pragma unroll
      for (int j = 0; j < 7; ++j) acc[i][j] = (f32x4){0.f, 0.f, 0.f, 0.f};
    constexpr int NKT = DM / G1_BK;
    issue(0);
    issue(1);
    const int q4 = (lane & 15) >> 2, p4 = lane & 3;
    bf16x4 tl[2][2][7], th[2][2][7];
    bf16x8 af[2][2][2];
    auto rdfrag = [&](auto ST) {
      constexpr int st = decltype(ST)::value;
      const char* A = smem + st * G1_STAGE;
      const unsigned baddr = lds_addr(smem) + st * G1_STAGE + G1_ABYTES + (fq * 4 + q4) * G1_BRS + p4 * 8;
      auto rd = [&](auto J) {
        constexpr int j = decltype(J)::value;
        tl[st][0][j] = tr_read<j * 32>(baddr);
        th[st][0][j] = tr_read<j * 32 + 16 * G1_BRS>(baddr);
        tl[st][1][j] = tr_read<j * 32 + 32 * G1_BRS>(baddr);
        th[st][1][j] = tr_read<j * 32 + 48 * G1_BRS>(baddr);
      };
      Unroll<0, 7>::run(rd);
#pragma unroll
      for (int ks = 0; ks < 2; ++ks)
#pragma unroll
        for (int i = 0; i < 2; ++i) {
          int row = w * 32 + i * 16 + fr;
          int c0 = ks * 4 + (fq >> 1), c1 = c0 + 2;
          bf16x4 x0 = *reinterpret_cast<const bf16x4*>(A + row * 128 + ((c0 ^ (row & 7)) << 4) + (fq & 1) * 8);
          bf16x4 x1 = *reinterpret_cast<const bf16x4*>(A + row * 128 + ((c1 ^ (row & 7)) << 4) + (fq & 1) * 8);
          af[st][ks][i] = __builtin_shufflevector(x0, x1, 0, 1, 2, 3, 4, 5, 6, 7);
        }
    };
    auto waitfrag = [&](auto ST) {
      constexpr int st = decltype(ST)::value;
      wait14(tl[st][0], th[st][0]);
      wait14(tl[st][1], th[st][1]);
    };
    auto mma = [&](auto ST) {
      constexpr int st = decltype(ST)::value;
#pragma unroll
      for (int ks = 0; ks < 2; ++ks)
#pragma unroll
        for (int j = 0; j < 7; ++j) {
          bf16x8 bfr = __builtin_shufflevector(tl[st][ks][j], th[st][ks][j], 0, 1, 2, 3, 4, 5, 6, 7);
#pragma unroll
          for (int i = 0; i < 2; ++i)
            acc[i][j] = __builtin_amdgcn_mfma_f32_16x16x32_bf16(bfr, af[st][ks][i], acc[i][j], 0, 0, 0);
        }
    };
    auto step = [&](auto ST, int kt) {
      constexpr int st = decltype(ST)::value;
      wait_vm<0>();
      __builtin_amdgcn_s_barrier();
      if (kt + 2 < NKT) issue(kt + 2);
      rdfrag(std::integral_constant<int, st ^ 1>{});
      mma(ST);
      waitfrag(std::integral_constant<int, st ^ 1>{});
    };
    wait_vm<8>();
    __builtin_amdgcn_s_barrier();
    rdfrag(std::integral_constant<int, 0>{});
    waitfrag(std::integral_constant<int, 0>{});
#pragma unroll 1
    for (int kt = 0; kt < 6; kt += 2) {
      step(std::integral_constant<int, 0>{}, kt);
      step(std::integral_constant<int, 1>{}, kt + 1);
    }
    mma(std::integral_constant<int, 0>{});
    STAMPK(1, 4);
    unsigned warm0, warm1 = 0;
    {
      const int o = (bid >> 3) * 50176 + tid * 128;
      warm0 = *reinterpret_cast<const unsigned*>(reinterpret_cast<const char*>(w2) + o);
      if (tid < 136) warm1 = *reinterpret_cast<const unsigned*>(reinterpret_cast<const char*>(w2) + o + 32768);
    }
    __builtin_amdgcn_sched_barrier(0);
    u32x2 pk[2][7];
#pragma unroll
    for (int i = 0; i < 2; ++i) {
#pragma unroll
      for (int j = 0; j < 7; ++j) {
#pragma unroll
        for (int r = 0; r < 2; ++r) {
          unsigned p0 = pk2bf(acc[i][j][2 * r], acc[i][j][2 * r + 1]);
          unsigned p1 = pk2bf(lo_f(p0) + lo_f(bbp[j][r]), hi_f(p0) + hi_f(bbp[j][r]));
          float t0 = __builtin_fmaxf(lo_f(p1), 0.f), t1 = __builtin_fmaxf(hi_f(p1), 0.f);
          pk[i][j][r] = pk2bf(t0 * t0, t1 * t1);
        }
      }
    }
#pragma unroll
    for (int i = 0; i < 2; ++i) {
      u16* hrow = hbuf + (size_t)(m0 + w * 32 + i * 16 + fr) * HS + n0;
#pragma unroll
      for (int P = 0; P < 3; ++P)
        *reinterpret_cast<u32x4*>(hrow + P * 32 + (fq & 1) * 16 + (fq >> 1) * 8) = pair_cols(pk[i][2 * P], pk[i][2 * P + 1]);
      *reinterpret_cast<u32x2*>(hrow + 96 + fq * 4) = pk[i][6];
    }
    asm volatile("" ::"v"(warm0), "v"(warm1));
    STAMPK(1, 5);
    return;
  }
  const int cb = BIDX - G1_BLOCKS;
  const int nch = (cb + 256 < DM) ? 2 : 1;
  char* XS = smem;
  char* KM = smem + CV_XBYTES;
  STAMPK(1, 0);
  u32x4 kv, cur[2], prv[2], xv;
  float dsk, kinv;
  auto prefetch = [&](int d) {
    const u16* xrow = xnT + (size_t)d * SL;
    kv = *reinterpret_cast<const u32x4*>(kexp + (size_t)d * SL + tid * 8);
#pragma unroll
    for (int rep = 0; rep < 2; ++rep) {
      int gg = tid + rep * 256 - 3;
      cur[rep] = (u32x4){0u, 0u, 0u, 0u}; prv[rep] = (u32x4){0u, 0u, 0u, 0u};
      if (rep == 0 || tid < 4) {
        if (gg >= 0 && gg < 256) cur[rep] = *reinterpret_cast<const u32x4*>(xrow + gg * 8);
        if (gg >= 1 && gg <= 256) prv[rep] = *reinterpret_cast<const u32x4*>(xrow + (gg - 1) * 8);
      }
    }
    xv = *reinterpret_cast<const u32x4*>(xrow + tid * 8);
    dsk = bf2f(dskip[d]);
    kinv = kinvp[d];
  };
  prefetch(cb);
  for (int i = tid; i < CV_KPAD * CV_KB / 16; i += 256)
    *reinterpret_cast<u32x4*>(KM + i * 16) = (u32x4){0u, 0u, 0u, 0u};
  for (int ch = 0; ch < nch; ++ch) {
    const int d = cb + ch * 256;
#pragma unroll
    for (int rep = 0; rep < 2; ++rep) {
      if (rep == 0 || tid < 4) {
        int gg = tid + rep * 256 - 3;
        int q = 256 - gg;
        unsigned Dw[8];
#pragma unroll
        for (int k = 0; k < 4; ++k) {
          Dw[k] = pk2h(lo_f(prv[rep][k]), hi_f(prv[rep][k]));
          Dw[4 + k] = pk2h(lo_f(cur[rep][k]), hi_f(cur[rep][k]));
        }
#pragma unroll
        for (int c = 0; c < 8; ++c) {
          u32x4 o;
#pragma unroll
          for (int k = 0; k < 4; ++k) {
            const int la = 8 + c - 2 * k;
            unsigned val;
            if (la & 1) {
              unsigned dd = Dw[(la - 1) >> 1];
              val = (dd >> 16) | (dd << 16);
            } else {
              val = (Dw[la >> 1] & 0xffffu) | (Dw[(la >> 1) - 1] & 0xffff0000u);
            }
            o[k] = val;
          }
          *reinterpret_cast<u32x4*>(XS + c * CV_XS + q * 16) = o;
        }
      }
    }
    {
      const int l0 = tid * 8;
      *reinterpret_cast<u32x4*>(KM + (CV_KPAD + (l0 >> 5)) * CV_KB + (l0 & 31) * 2) = kv;
    }
    const u32x4 xv_c = xv;
    const float dsk_c = dsk, kinv_c = kinv;
    bsync();
    if (ch == 0) STAMPK(1, 2);
    if (ch + 1 < nch) prefetch(d + 256);
    f32x16 acc0, acc1;
#pragma unroll
    for (int i = 0; i < 16; ++i) { acc0[i] = 0.f; acc1[i] = 0.f; }
    {
      const int t0 = lane & 31, hh = lane >> 5;
      const int c = t0 & 7;
      const char* abase = XS + c * CV_XS + 2 * (2048 - 8 * (t0 >> 3) + 8 * hh);
      const char* bbase = KM + (CV_KPAD + t0) * CV_KB + 16 * hh;
      f16x8 fa0[2][2], fa1[2][2], fb0[2][2], fb1[2][2];
      auto ldf = [&](int set, int it) {
        const int dl = w + 4 * it;
#pragma unroll
        for (int ks = 0; ks < 2; ++ks) {
          fa0[set][ks] = *reinterpret_cast<const f16x8*>(abase - 64 * dl + 32 * ks);
          fa1[set][ks] = *reinterpret_cast<const f16x8*>(abase - 64 * (dl + 32) + 32 * ks);
          const char* b0p = bbase - dl * CV_KB + 32 * ks;
          fb0[set][ks] = *reinterpret_cast<const f16x8*>(b0p);
          fb1[set][ks] = *reinterpret_cast<const f16x8*>(b0p + 32 * CV_KB);
        }
      };
      auto mm = [&](int set) {
#pragma unroll
        for (int ks = 0; ks < 2; ++ks) {
          acc0 = __builtin_amdgcn_mfma_f32_32x32x16_f16(fa0[set][ks], fb0[set][ks], acc0, 0, 0, 0);
          acc1 = __builtin_amdgcn_mfma_f32_32x32x16_f16(fa0[set][ks], fb1[set][ks], acc1, 0, 0, 0);
          acc1 = __builtin_amdgcn_mfma_f32_32x32x16_f16(fa1[set][ks], fb0[set][ks], acc1, 0, 0, 0);
        }
      };
      ldf(0, 0);
#pragma unroll 1
      for (int it = 0; it < 6; it += 2) {
        ldf(1, it + 1);
        __builtin_amdgcn_sched_barrier(0);
        mm(0);
        __builtin_amdgcn_sched_barrier(0);
        ldf(0, it + 2);
        __builtin_amdgcn_sched_barrier(0);
        mm(1);
        __builtin_amdgcn_sched_barrier(0);
      }
      ldf(1, 7);
      __builtin_amdgcn_sched_barrier(0);
      mm(0);
      mm(1);
    }
    bsync();
    if (ch == 0) STAMPK(1, 3);
    float* red = reinterpret_cast<float*>(smem);
    {
      const int col = lane & 31, hh = lane >> 5;
#pragma unroll
      for (int g = 0; g < 4; ++g) {
        f32x4 v0 = (f32x4){acc0[4 * g], acc0[4 * g + 1], acc0[4 * g + 2], acc0[4 * g + 3]};
        f32x4 v1 = (f32x4){acc1[4 * g], acc1[4 * g + 1], acc1[4 * g + 2], acc1[4 * g + 3]};
        *reinterpret_cast<f32x4*>(red + w * 2304 + 36 * col + 8 * g + 4 * hh) = v0;
        *reinterpret_cast<f32x4*>(red + w * 2304 + 36 * (32 + col) + 8 * g + 4 * hh) = v1;
      }
    }
    bsync();
    {
      const int t = tid * 8;
      f32x4 y0 = (f32x4){0.f, 0.f, 0.f, 0.f}, y1 = (f32x4){0.f, 0.f, 0.f, 0.f};
#pragma unroll
      for (int ww = 0; ww < 4; ++ww) {
        y0 += *reinterpret_cast<const f32x4*>(red + ww * 2304 + 36 * (tid >> 2) + 8 * (tid & 3));
        y1 += *reinterpret_cast<const f32x4*>(red + ww * 2304 + 36 * (tid >> 2) + 8 * (tid & 3) + 4);
      }
      u32x4 o;
#pragma unroll
      for (int j = 0; j < 4; ++j) {
        float ya = (j < 2 ? y0[2 * j] : y1[2 * j - 4]) * kinv_c;
        float yb = (j < 2 ? y0[2 * j + 1] : y1[2 * j - 3]) * kinv_c;
        o[j] = pk2bf(rbf(ya) + rbf(lo_f(xv_c[j]) * dsk_c), rbf(yb) + rbf(hi_f(xv_c[j]) * dsk_c));
      }
      *reinterpret_cast<u32x4*>(sT + (size_t)d * SL + t) = o;
    }
    if (ch == 0) STAMPK(1, 4);
    if (ch + 1 < nch) bsync();
  }

  STAMPK(1, 5);
}

constexpr int G2_BM = 64, G2_BN = 64, G2_BK = 128;
constexpr int G2_BLOCKS = (SL / G2_BM) * (DM / G2_BN);
constexpr int G2_ABYTES = G2_BM * G2_BK * 2;
constexpr int G2_STAGE = 32768;
#define G2NS 4
constexpr int G2_NS = G2NS;
constexpr int K2_LDS = G2_NS * G2_STAGE;

__device__ __forceinline__ void wait8(bf16x4 (&a)[2][2][2]) {
  asm volatile("s_waitcnt lgkmcnt(0)"
               : "+v"(a[0][0][0]), "+v"(a[0][0][1]), "+v"(a[0][1][0]), "+v"(a[0][1][1]),
                 "+v"(a[1][0][0]), "+v"(a[1][0][1]), "+v"(a[1][1][0]), "+v"(a[1][1][1]));
}
__device__ __forceinline__ int g2_bswz(int r) { return ((r >> 1) & 3) << 1; }

__global__ __launch_bounds__(256, 1) NOPK_ATTR void k2_gemm2(
    const u16* __restrict__ hbuf, const u16* __restrict__ w2, const u16* __restrict__ b2,
    const u16* __restrict__ u, const u16* __restrict__ sT, u16* __restrict__ out) {
  extern __shared__ __attribute__((aligned(16))) char smem[];
  const int tid = TIDX, lane = tid & 63, w = tid >> 6;
  const int bid = BIDX;
  const int xcd = bid & 7, rest = bid >> 3;
  const int mt_i = xcd * 4 + (rest & 3);
  const int nt_i = rest >> 2;
  const int m0 = mt_i * G2_BM, n0 = nt_i * G2_BN;
  const int fr = lane & 15, fq = lane >> 4;
  const int wm = w & 1, wn = w >> 1;
  STAMPK(2, 0);
  const int ncol = wn * 32 + (fq & 1) * 16 + (fq >> 1) * 8;
  u32x4 e_u[2];
  u16 e_s[2][8];
#pragma unroll
  for (int i = 0; i < 2; ++i) {
    const int m = m0 + wm * 32 + i * 16 + fr, n = n0 + ncol;
    e_u[i] = *reinterpret_cast<const u32x4*>(u + (size_t)m * DM + n);
#pragma unroll
    for (int r = 0; r < 8; ++r) e_s[i][r] = sT[(size_t)(n + r) * SL + m];
  }
  float bb[2][4];
#pragma unroll
  for (int j = 0; j < 2; ++j) {
    u32x2 t = *reinterpret_cast<const u32x2*>(b2 + n0 + wn * 32 + j * 16 + fq * 4);
    bb[j][0] = lo_f(t[0]); bb[j][1] = hi_f(t[0]); bb[j][2] = lo_f(t[1]); bb[j][3] = hi_f(t[1]);
  }
  const u16* srcA[2];
  const u16* srcB[2];
#pragma unroll
  for (int i = 0; i < 2; ++i) {
    int p = (2 * w + i) * 64 + lane;
    int row = p >> 3, ph = p & 7;
    srcA[i] = hbuf + (size_t)(m0 + row) * HS + (ph ^ (row & 7)) * 8;
    srcB[i] = w2 + (size_t)row * DM + n0 + (ph ^ g2_bswz(row)) * 8;
  }
  auto issue = [&](int kt) {
    char* base = smem + (kt % G2_NS) * G2_STAGE + (2 * w) * 1024 + lane * 16;
#pragma unroll
    for (int hf = 0; hf < 2; ++hf)
#pragma unroll
      for (int i = 0; i < 2; ++i) {
        __builtin_amdgcn_global_load_lds((const unsigned*)(srcA[i] + kt * G2_BK + hf * 64),
                                         (unsigned*)(base + hf * 8192 + i * 1024), 16, 0, 0);
        __builtin_amdgcn_global_load_lds((const unsigned*)(srcB[i] + (size_t)(kt * G2_BK + hf * 64) * DM),
                                         (unsigned*)(base + G2_ABYTES + hf * 8192 + i * 1024), 16, 0, 0);
      }
  };
  f32x4 acc[2][2];
#pragma unroll
  for (int i = 0; i < 2; ++i)
#pragma unroll
    for (int j = 0; j < 2; ++j) acc[i][j] = (f32x4){0.f, 0.f, 0.f, 0.f};
  constexpr int NKT = FF / G2_BK;
#pragma unroll
  for (int s0 = 0; s0 < G2_NS - 1; ++s0) issue(s0);
  const int q4 = (lane & 15) >> 2, p4 = lane & 3;
  bf16x4 t[2][2][2][2][2];
  bf16x4 xa[2][2][2][2][2];
  auto rdfrag = [&](auto KT) {
    constexpr int kt = decltype(KT)::value;
    constexpr int st = kt & 1;
    const char* A = smem + (kt % G2_NS) * G2_STAGE;
    const unsigned bbase = lds_addr(smem) + (kt % G2_NS) * G2_STAGE + G2_ABYTES;
#pragma unroll
    for (int j = 0; j < 2; ++j)
#pragma unroll
      for (int hf = 0; hf < 2; ++hf) {
        const int r0 = fq * 4 + q4 + 16 * hf;
        const int ch = (wn * 2 + j) * 2 + (p4 >> 1);
        const unsigned a0 = bbase + r0 * 128 + ((ch ^ g2_bswz(r0)) << 4) + (p4 & 1) * 8;
        t[st][0][0][j][hf] = tr_read<0>(a0);
        t[st][0][1][j][hf] = tr_read<32 * 128>(a0);
        t[st][1][0][j][hf] = tr_read<64 * 128>(a0);
        t[st][1][1][j][hf] = tr_read<96 * 128>(a0);
      }
#pragma unroll
    for (int ks = 0; ks < 2; ++ks)
#pragma unroll
      for (int i = 0; i < 2; ++i)
#pragma unroll
        for (int hf = 0; hf < 2; ++hf) {
          int row = wm * 32 + i * 16 + fr;
          int c0 = ks * 4 + (fq >> 1) + 2 * hf;
          const unsigned a0 = lds_addr(A) + row * 128 + ((c0 ^ (row & 7)) << 4) + (fq & 1) * 8;
          xa[st][0][ks][i][hf] = ld_b64<0>(a0);
          xa[st][1][ks][i][hf] = ld_b64<8192>(a0);
        }
  };
  auto waitfrag = [&](auto ST) {
    constexpr int st = decltype(ST)::value;
    wait8(t[st][0]);
    wait8(t[st][1]);
    wait8(xa[st][0]);
    wait8(xa[st][1]);
  };
  wait_vm<8 * (G2_NS - 2)>();
  __builtin_amdgcn_s_barrier();
  STAMPK(2, 1);
  rdfrag(std::integral_constant<int, 0>{});
  waitfrag(std::integral_constant<int, 0>{});
  auto step = [&](auto KT) {
    constexpr int kt = decltype(KT)::value;
    constexpr int st = kt & 1;
    if constexpr (kt + 1 < NKT) {
      constexpr int last = (kt + G2_NS - 2) < (NKT - 1) ? (kt + G2_NS - 2) : (NKT - 1);
      wait_vm<8 * (last - (kt + 1))>();
      __builtin_amdgcn_s_barrier();
      if constexpr (kt == 3) STAMPK(2, 2);
      if constexpr (kt == 7) STAMPK(2, 3);
      if constexpr (kt == 11) STAMPK(2, 4);
      if constexpr (kt + G2_NS - 1 < NKT) issue(kt + G2_NS - 1);
      rdfrag(std::integral_constant<int, kt + 1>{});
    }
#pragma unroll
    for (int hv = 0; hv < 2; ++hv)
#pragma unroll
      for (int ks = 0; ks < 2; ++ks)
#pragma unroll
        for (int j = 0; j < 2; ++j) {
          bf16x8 bfr = __builtin_shufflevector(t[st][hv][ks][j][0], t[st][hv][ks][j][1], 0, 1, 2, 3, 4, 5, 6, 7);
#pragma unroll
          for (int i = 0; i < 2; ++i)
            acc[i][j] = __builtin_amdgcn_mfma_f32_16x16x32_bf16(
                bfr, __builtin_shufflevector(xa[st][hv][ks][i][0], xa[st][hv][ks][i][1], 0, 1, 2, 3, 4, 5, 6, 7),
                acc[i][j], 0, 0, 0);
        }
    if constexpr (kt + 1 < NKT) waitfrag(std::integral_constant<int, st ^ 1>{});
  };
  Unroll<0, NKT>::run(step);
  STAMPK(2, 5);
#pragma unroll
  for (int i = 0; i < 2; ++i) {
    const int m = m0 + wm * 32 + i * 16 + fr, n = n0 + ncol;
    u32x2 gp[2];
#pragma unroll
    for (int j = 0; j < 2; ++j) {
#pragma unroll
      for (int r = 0; r < 2; ++r) {
        unsigned p0 = pk2bf(acc[i][j][2 * r], acc[i][j][2 * r + 1]);
        unsigned p1 = pk2bf(lo_f(p0) + bb[j][2 * r], hi_f(p0) + bb[j][2 * r + 1]);
        float z0 = lo_f(p1), z1 = hi_f(p1);
        gp[j][r] = pk2bf(silu_f(z0), silu_f(z1));
      }
    }
    const u32x4 gv = pair_cols(gp[0], gp[1]);
    u32x4 o;
#pragma unroll
    for (int e = 0; e < 4; ++e)
      o[e] = pk2bf(lo_f(e_u[i][e]) + rbf(bf2f(e_s[i][2 * e]) * lo_f(gv[e])),
                   hi_f(e_u[i][e]) + rbf(bf2f(e_s[i][2 * e + 1]) * hi_f(gv[e])));
    *reinterpret_cast<u32x4*>(out + (size_t)m * DM + n) = o;
  }
  STAMPK(2, 6);
}

#if defined(__HIP_DEVICE_COMPILE__)
#pragma clang attribute pop
#endif
extern "C" void kernel_launch(void* const* d_in, const int* in_sizes, int n_in,
                              void* d_out, int out_size, void* d_ws, size_t ws_size,
                              hipStream_t stream) {
  const u16* u = (const u16*)d_in[0];
  const u16* logA = (const u16*)d_in[1];
  const u16* Aim = (const u16*)d_in[2];
  const u16* Cre = (const u16*)d_in[3];
  const u16* Cim = (const u16*)d_in[4];
  const u16* logdt = (const u16*)d_in[5];
  const u16* dskip = (const u16*)d_in[6];
  const u16* w1 = (const u16*)d_in[8];
  const u16* b1 = (const u16*)d_in[9];
  const u16* w2 = (const u16*)d_in[10];
  const u16* b2 = (const u16*)d_in[11];
  u16* out = (u16*)d_out;
  char* ws = (char*)d_ws;
  u16* xn = (u16*)(ws);
  u16* xnT = (u16*)(ws + (2u << 20));
  u16* sT = (u16*)(ws + (4u << 20));
  u16* kexp = (u16*)(ws + (6u << 20));
  float* kinvp = (float*)(ws + (7u << 20) + (900u << 10));
  u16* hbuf = (u16*)(ws + (8u << 20));

  hipFuncSetAttribute((const void*)k0_ln_kgen, hipFuncAttributeMaxDynamicSharedMemorySize, K0_LDS);
  hipFuncSetAttribute((const void*)k1_gemm1_conv, hipFuncAttributeMaxDynamicSharedMemorySize, K1_LDS);
  hipFuncSetAttribute((const void*)k2_gemm2, hipFuncAttributeMaxDynamicSharedMemorySize, K2_LDS);
  k0_ln_kgen<<<LN_BLOCKS + DM, 256, K0_LDS, stream>>>(u, logA, Aim, Cre, Cim, logdt, xn, xnT, kexp, kinvp, w1);
  k1_gemm1_conv<<<G1_BLOCKS + 256, 256, K1_LDS, stream>>>(xn, xnT, kexp, kinvp, w1, b1, dskip, hbuf, sT, w2);
  k2_gemm2<<<G2_BLOCKS, 256, K2_LDS, stream>>>(hbuf, w2, b2, u, sT, out);
}
```

```cpp
#include <hip/hip_runtime.h>
#include <hip/hip_bf16.h>
#include <stdint.h>
#include <type_traits>

#if defined(__HIP_DEVICE_COMPILE__)
#pragma clang attribute push(__attribute__((target("no-packed-fp32-ops"))), apply_to = function)
#endif
typedef unsigned short u16;
typedef __attribute__((ext_vector_type(8))) short bf16x8;
typedef __attribute__((ext_vector_type(4))) short bf16x4;
typedef __attribute__((ext_vector_type(8))) _Float16 f16x8;
typedef __attribute__((ext_vector_type(2))) _Float16 f16x2;
typedef __attribute__((ext_vector_type(2))) __bf16 bf16x2;
typedef __attribute__((ext_vector_type(2))) float f32x2;
typedef __attribute__((ext_vector_type(4))) float f32x4;
typedef __attribute__((ext_vector_type(16))) float f32x16;
typedef __attribute__((ext_vector_type(4))) unsigned int u32x4;
typedef __attribute__((ext_vector_type(2))) unsigned int u32x2;
#define LDS_AS __attribute__((address_space(3)))

#define TIDX ((int)__builtin_amdgcn_workitem_id_x())
#define BIDX ((int)__builtin_amdgcn_workgroup_id_x())
__device__ __forceinline__ float u2f(unsigned v) { return __builtin_bit_cast(float, v); }
__device__ __forceinline__ unsigned f2u(float v) { return __builtin_bit_cast(unsigned, v); }
__device__ __forceinline__ void bsync() {
  __builtin_amdgcn_fence(__ATOMIC_RELEASE, "workgroup");
  __builtin_amdgcn_s_barrier();
  __builtin_amdgcn_fence(__ATOMIC_ACQUIRE, "workgroup");
}
__device__ __forceinline__ float shfl_xor_f(float v, int m, int lane) {
  return u2f((unsigned)__builtin_amdgcn_ds_bpermute((lane ^ m) << 2, (int)f2u(v)));
}
__device__ __forceinline__ float exp_acc(float x) {
  const float L2E_HI = 1.44269502162933349609375f, L2E_LO = 1.92596299112661746e-8f;
  float t = x * L2E_HI;
  float e = __builtin_fmaf(x, L2E_HI, -t) + x * L2E_LO;
  float r = __builtin_amdgcn_exp2f(t);
  return __builtin_fmaf(r, e * 0.693147180559945309f, r);
}

__device__ __forceinline__ float silu_f(float z) {
  float dn = 1.0f + exp_acc(-z);
  float r = __builtin_amdgcn_rcpf(dn);
  float q = z * r;
  float e = __builtin_fmaf(-q, dn, z);
  float res = __builtin_fmaf(e, r, q);
  return (dn < 3.0e38f) ? res : -0.0f * 0.f + (z * 0.f);
}

constexpr int SL = 2048;
constexpr int DM = 448;
constexpr int NMODE = 256;
constexpr int FF = 1792;
#define HPAD 64
constexpr int HS = FF + HPAD;
constexpr int KTAB = 48 * 16;

__device__ __forceinline__ float bf2f(u16 v) { return u2f(((unsigned)v) << 16); }
__device__ __forceinline__ unsigned pk2bf(float a, float b) {
  f32x2 v = {a, b};
  bf16x2 r = __builtin_convertvector(v, bf16x2);
  return *reinterpret_cast<unsigned*>(&r);
}
__device__ __forceinline__ u16 f2bf(float f) { return (u16)(pk2bf(f, 0.f) & 0xffffu); }
__device__ __forceinline__ float rbf(float f) { return u2f(pk2bf(f, 0.f) << 16); }
__device__ __forceinline__ float lo_f(unsigned v) { return u2f(v << 16); }
__device__ __forceinline__ float hi_f(unsigned v) { return u2f(v & 0xffff0000u); }
__device__ __forceinline__ unsigned pk2h(float a, float b) {
  f32x2 v = {a, b};
  f16x2 r = __builtin_convertvector(v, f16x2);
  return *reinterpret_cast<unsigned*>(&r);
}

template <int I, int N> struct Unroll {
  template <class F> static __device__ __forceinline__ void run(F& f) {
    f(std::integral_constant<int, I>{});
    Unroll<I + 1, N>::run(f);
  }
};
template <int N> struct Unroll<N, N> {
  template <class F> static __device__ __forceinline__ void run(F&) {}
};
template <int N> __device__ __forceinline__ void wait_vm() {
  asm volatile("s_waitcnt vmcnt(%0)" ::"n"(N) : "memory");
}
__device__ __forceinline__ void wait_lgkm0() { asm volatile("s_waitcnt lgkmcnt(0)" ::: "memory"); }

template <int OFF> __device__ __forceinline__ bf16x4 tr_read(unsigned addr) {
  bf16x4 r;
  asm volatile("ds_read_b64_tr_b16 %0, %1 offset:%2" : "=v"(r) : "v"(addr), "n"(OFF));
  return r;
}
__device__ __forceinline__ void wait14(bf16x4 (&a)[7], bf16x4 (&b)[7]) {
  asm volatile("s_waitcnt lgkmcnt(0)"
               : "+v"(a[0]), "+v"(a[1]), "+v"(a[2]), "+v"(a[3]), "+v"(a[4]), "+v"(a[5]), "+v"(a[6]),
                 "+v"(b[0]), "+v"(b[1]), "+v"(b[2]), "+v"(b[3]), "+v"(b[4]), "+v"(b[5]), "+v"(b[6]));
}
template <int OFF> __device__ __forceinline__ bf16x4 ld_b64(unsigned addr) {
  bf16x4 r;
  asm volatile("ds_read_b64 %0, %1 offset:%2" : "=v"(r) : "v"(addr), "n"(OFF));
  return r;
}
__device__ __forceinline__ unsigned lds_addr(const void* p) { return (unsigned)(size_t)p; }

#define STAMPK(k, s)
__device__ __forceinline__ u32x4 pair_cols(u32x2 a, u32x2 b) {
  auto r0 = __builtin_amdgcn_permlane16_swap(a[0], b[0], false, false);
  auto r1 = __builtin_amdgcn_permlane16_swap(a[1], b[1], false, false);
  return (u32x4){r0[0], r1[0], r0[1], r1[1]};
}
struct cf { float r, i; };
__device__ __forceinline__ cf cmul(cf a, cf b) {
  cf o; o.r = a.r * b.r - a.i * b.i; o.i = a.r * b.i + a.i * b.r; return o;
}
__device__ __forceinline__ cf cexp_p(float ar, float ai, float p) {
  float m = __builtin_amdgcn_exp2f((ar * p) * 1.4426950408889634f);
  float rev = (ai * p) * 0.15915494309189535f;
  rev = __builtin_amdgcn_fractf(rev);
  cf o; o.r = m * __builtin_amdgcn_cosf(rev); o.i = m * __builtin_amdgcn_sinf(rev); return o;
}
template <int CTRL> __device__ __forceinline__ float dpp_add(float s) {
  int t = __builtin_amdgcn_update_dpp(0, (int)f2u(s), CTRL, 0xf, 0xf, true);
  return s + u2f((unsigned)t);
}
__device__ __forceinline__ float red16(float s) {
  s = dpp_add<0xB1>(s);
  s = dpp_add<0x4E>(s);
  s = dpp_add<0x141>(s);
  s = dpp_add<0x140>(s);
  return s;
}

constexpr int LN_ROWS = 16;
constexpr int LN_BLOCKS = SL / LN_ROWS;
constexpr int KG_RS = 136;
constexpr int K0_LDS = 4 * 2 * 16 * KG_RS * 2 + 64;

__device__ __forceinline__ int ktab_index(int l) {
  int pos, r, b;
  if (l < 256) { pos = l; r = ((pos >> 7) << 3) + (pos & 7); b = (pos >> 3) & 15; return r * 16 + b; }
  pos = (int)rbf((float)l);
  if (pos < 512) { int q = pos - 256; r = 16 + ((q >> 7) << 2) + ((q >> 1) & 3); b = (q >> 3) & 15; }
  else if (pos < 1024) { int q = pos - 512; r = 24 + ((q >> 7) << 1) + ((q >> 2) & 1); b = (q >> 3) & 15; }
  else if (pos < 2048) { int q = pos - 1024; r = 32 + (q >> 7); b = (q >> 3) & 15; }
  else { r = 40; b = 0; }
  return r * 16 + b;
}

#define NOPK_ATTR
__global__ __launch_bounds__(256) NOPK_ATTR void k0_ln_kgen(
    const u16* __restrict__ u, const u16* __restrict__ logA, const u16* __restrict__ Aim,
    const u16* __restrict__ Cre, const u16* __restrict__ Cim, const u16* __restrict__ logdt,
    u16* __restrict__ xn, u16* __restrict__ xnT, u16* __restrict__ kexp, float* __restrict__ kinvp,
    const u16* __restrict__ w1, const char* __restrict__ wsb, const char* __restrict__ outb,
    const char* __restrict__ w2b) {
  extern __shared__ __attribute__((aligned(16))) char smem[];
  const int tid = TIDX, lane = tid & 63, w = tid >> 6;
  if (BIDX < LN_BLOCKS) {
    u16* tile = reinterpret_cast<u16*>(smem);
    constexpr int TS = DM + 8;
    STAMPK(0, 0);
    const int rb = tid >> 4, l16 = tid & 15;
    const int rblk = ((BIDX & 7) << 4) + (BIDX >> 3);
    const int row = rblk * LN_ROWS + rb;
    u32x2 v[7];
#pragma unroll
    for (int i = 0; i < 7; ++i)
      v[i] = *reinterpret_cast<const u32x2*>(u + (size_t)row * DM + i * 64 + l16 * 4);
    float x[28];
#pragma unroll
    for (int i = 0; i < 7; ++i) {
      x[4 * i] = lo_f(v[i][0]); x[4 * i + 1] = hi_f(v[i][0]);
      x[4 * i + 2] = lo_f(v[i][1]); x[4 * i + 3] = hi_f(v[i][1]);
    }
    float s = 0.f;
#pragma unroll
    for (int j = 0; j < 28; ++j) s += x[j];
    s = red16(s);
    const float mean = s * (1.0f / DM);
    float q = 0.f;
#pragma unroll
    for (int j = 0; j < 28; ++j) { float dd = x[j] - mean; q += dd * dd; }
    q = red16(q);
    const float var_b = rbf(q * (1.0f / DM));
    const float mean_b = rbf(mean);
    const float den = rbf(__builtin_sqrtf(rbf(var_b + 1e-5f)));
#pragma unroll
    for (int i = 0; i < 7; ++i) {
      u32x2 o;
      o[0] = pk2bf(rbf(x[4 * i] - mean_b) / den, rbf(x[4 * i + 1] - mean_b) / den);
      o[1] = pk2bf(rbf(x[4 * i + 2] - mean_b) / den, rbf(x[4 * i + 3] - mean_b) / den);
      *reinterpret_cast<u32x2*>(xn + (size_t)row * DM + i * 64 + l16 * 4) = o;
      *reinterpret_cast<u32x2*>(tile + rb * TS + i * 64 + l16 * 4) = o;
    }
    bsync();
    STAMPK(0, 1);
    for (int d = tid; d < DM; d += 256) {
      unsigned pk[8];
#pragma unroll
      for (int r = 0; r < 8; ++r)
        pk[r] = (unsigned)tile[(2 * r) * TS + d] | ((unsigned)tile[(2 * r + 1) * TS + d] << 16);
      u32x4* dst = reinterpret_cast<u32x4*>(xnT + (size_t)d * SL + rblk * LN_ROWS);
      dst[0] = (u32x4){pk[0], pk[1], pk[2], pk[3]};
      dst[1] = (u32x4){pk[4], pk[5], pk[6], pk[7]};
    }
    {
      unsigned wv[4];
      const int o = (BIDX >> 3) * 100352 + tid * 128;
#pragma unroll
      for (int q = 0; q < 4; ++q) {
        wv[q] = 0;
        if (q < 3 || tid < 16) wv[q] = *reinterpret_cast<const unsigned*>(reinterpret_cast<const char*>(w1) + o + q * 32768);
      }
      asm volatile("" ::"v"(wv[0]), "v"(wv[1]), "v"(wv[2]), "v"(wv[3]));
      unsigned tv = 0;
      if (tid < 192) tv = *reinterpret_cast<const unsigned*>(wsb + (4u << 20) + tid * 65536);
      else if (tid < 220) tv = *reinterpret_cast<const unsigned*>(outb + (tid - 192) * 65536);
      else if (tid < 244) tv = *reinterpret_cast<const unsigned*>(w2b + (tid - 220) * 65536);
      asm volatile("" ::"v"(tv));
    }
    STAMPK(0, 5);
    return;
  }
  const int d = BIDX - LN_BLOCKS;
  u16* wbase = reinterpret_cast<u16*>(smem) + w * (2 * 16 * KG_RS);
  u16* Vb = wbase;
  u16* Wb = wbase + 16 * KG_RS;
  STAMPK(0, 0);
  const int idx = d * NMODE + tid;
  const u16 r_ldt = logdt[d], r_la = logA[idx], r_ai = Aim[idx], r_cr = Cre[idx], r_ci = Cim[idx];
  const float dt = rbf(exp_acc(bf2f(r_ldt)));
  const float Are = -rbf(exp_acc(bf2f(r_la)));
  const float ar = Are * dt;
  const float ai = bf2f(r_ai) * dt;
  cf C2; C2.r = 2.0f * (bf2f(r_cr) * dt); C2.i = 2.0f * (bf2f(r_ci) * dt);
  float* RM = reinterpret_cast<float*>(smem + 4 * 2 * 16 * KG_RS * 2);
  float wsc, wsc_inv;
  {
    float m = __builtin_fmaxf(__builtin_fabsf(C2.r), __builtin_fabsf(C2.i));
    float sm = __builtin_fabsf(C2.r) + __builtin_fabsf(C2.i);
#pragma unroll
    for (int o = 32; o > 0; o >>= 1) { m = __builtin_fmaxf(m, shfl_xor_f(m, o, lane)); sm += shfl_xor_f(sm, o, lane); }
    if (lane == 0) RM[w] = sm;
    int e = (int)((f2u(m) >> 23) & 0xff) - 127;
    if (m == 0.f || e > 100 || e < -100) e = 13;
    wsc = u2f((unsigned)(127 + 13 - e) << 23);
    wsc_inv = u2f((unsigned)(127 - 13 + e) << 23);
  }
  C2.r *= wsc; C2.i *= wsc;
  auto st = [&](u16* buf, int row, float re, float im) {
    *reinterpret_cast<unsigned*>(buf + row * KG_RS + 2 * lane) = pk2h(re, im);
  };
  {
    cf V1 = cexp_p(ar, ai, 8.f);
    cf V8 = cexp_p(ar, ai, 64.f);
    cf V[8];
    V[0].r = 1.f; V[0].i = 0.f;
    V[1] = V1;
    V[2] = cmul(V1, V1);
    V[3] = cmul(V[2], V1);
    V[4] = cmul(V[2], V[2]);
    V[5] = cmul(V[4], V1);
    V[6] = cmul(V[4], V[2]);
    V[7] = cmul(V[4], V[3]);
#pragma unroll
    for (int b = 0; b < 8; ++b) st(Vb, b, V[b].r, V[b].i);
    st(Vb, 8, V8.r, V8.i);
#pragma unroll
    for (int b = 1; b < 8; ++b) { cf t = cmul(V8, V[b]); st(Vb, 8 + b, t.r, t.i); }
  }
  cf U[8];
  {
    cf E1 = cexp_p(ar, ai, 1.f);
    cf E2 = cmul(E1, E1);
    cf E3 = cmul(E2, E1);
    cf E4 = cmul(E2, E2);
    U[0] = C2;
    U[1] = cmul(C2, E1);
    U[2] = cmul(C2, E2);
    U[3] = cmul(C2, E3);
    U[4] = cmul(C2, E4);
    U[5] = cmul(U[4], E1);
    U[6] = cmul(U[4], E2);
    U[7] = cmul(U[4], E3);
  }
  const cf F1 = cexp_p(ar, ai, 128.f);
  const cf F2 = cexp_p(ar, ai, 256.f);
  const cf F4 = cexp_p(ar, ai, 512.f);
  const cf F8 = cexp_p(ar, ai, 1024.f);
  const cf F3 = cmul(F2, F1);

  f32x4 acc[3];
#pragma unroll
  for (int mt = 0; mt < 3; ++mt) acc[mt] = (f32x4){0.f, 0.f, 0.f, 0.f};
  const int fr = lane & 15, fq = lane >> 4;
  asm volatile("" ::: "memory");
  f16x8 bv[4];
#pragma unroll
  for (int s4 = 0; s4 < 4; ++s4)
    bv[s4] = *reinterpret_cast<const f16x8*>(Vb + fr * KG_RS + 32 * s4 + 8 * fq);
  auto tile = [&](int mt, const cf* T) {
    asm volatile("" ::: "memory");
#pragma unroll
    for (int r = 0; r < 16; ++r) st(Wb, r, T[r].r, -T[r].i);
    asm volatile("" ::: "memory");
#pragma unroll
    for (int s4 = 0; s4 < 4; ++s4) {
      f16x8 a = *reinterpret_cast<const f16x8*>(Wb + fr * KG_RS + 32 * s4 + 8 * fq);
      acc[mt] = __builtin_amdgcn_mfma_f32_16x16x32_f16(a, bv[s4], acc[mt], 0, 0, 0);
    }
    asm volatile("" ::: "memory");
  };
  cf T[16];
#pragma unroll
  for (int e = 0; e < 8; ++e) { T[e] = U[e]; T[8 + e] = cmul(F1, U[e]); }
  STAMPK(0, 1);
  tile(0, T);
  STAMPK(0, 2);
  cf G[4];
  G[0] = F4; G[1] = cmul(F4, F1); G[2] = cmul(F4, F2); G[3] = cmul(F4, F3);
#pragma unroll
  for (int e = 0; e < 4; ++e) {
    T[e] = cmul(F2, U[2 * e]);
    T[4 + e] = cmul(F3, U[2 * e]);
    T[8 + 2 * e] = cmul(G[e], U[0]);
    T[8 + 2 * e + 1] = cmul(G[e], U[4]);
  }
  tile(1, T);
  {
    cf H0 = cmul(F8, U[0]);
    T[0] = H0; T[1] = cmul(H0, F1); T[2] = cmul(H0, F2); T[3] = cmul(H0, F3);
#pragma unroll
    for (int a2 = 0; a2 < 4; ++a2) T[4 + a2] = cmul(H0, G[a2]);
    T[8] = cmul(H0, F8);
#pragma unroll
    for (int r = 9; r < 16; ++r) { T[r].r = 0.f; T[r].i = 0.f; }
  }
  tile(2, T);
#pragma unroll
  for (int mt = 0; mt < 3; ++mt) acc[mt] *= wsc_inv;
  bsync();
  STAMPK(0, 3);
  float* red = reinterpret_cast<float*>(smem);
#pragma unroll
  for (int mt = 0; mt < 3; ++mt)
#pragma unroll
    for (int j = 0; j < 4; ++j) red[w * KTAB + (mt * 16 + fq * 4 + j) * 16 + fr] = acc[mt][j];
  bsync();
  float* KT = red + 4 * KTAB;
#pragma unroll
  for (int r = 0; r < 3; ++r) {
    int i = tid + r * 256;
    KT[i] = (red[i] + red[KTAB + i]) + (red[2 * KTAB + i] + red[3 * KTAB + i]);
  }
  bsync();
  {
    float m = (RM[0] + RM[1]) + (RM[2] + RM[3]);
    int e = (int)((f2u(m) >> 23) & 0xff) - 127;
    if (m == 0.f || e > 100 || e < -100) e = 14;
    const float kscale = u2f((unsigned)(127 + 14 - e) << 23);
    if (tid == 0) kinvp[d] = u2f((unsigned)(127 - 14 + e) << 23);
    const int l0 = tid * 8;
    u32x4 hv;
#pragma unroll
    for (int j = 0; j < 4; ++j) {
      float v0 = KT[ktab_index(l0 + 2 * j)], v1 = KT[ktab_index(l0 + 2 * j + 1)];
      hv[j] = pk2h(v0 * kscale, v1 * kscale);
    }
    *reinterpret_cast<u32x4*>(kexp + (size_t)d * SL + l0) = hv;
  }

  STAMPK(0, 5);
}

constexpr int G1_BM = 128, G1_BN = 112, G1_BK = 64;
constexpr int G1_BLOCKS = (SL / G1_BM) * (FF / G1_BN);
constexpr int G1_ABYTES = G1_BM * G1_BK * 2;
constexpr int G1_BRS = 224;
constexpr int G1_STAGE = 32768;
constexpr int G1_NS = 2;
constexpr int CV_XS = 4160;
constexpr int CV_XBYTES = 36864;
constexpr int CV_KB = 80;
constexpr int CV_KPAD = 31;
constexpr int CV_KBYTES = (CV_KPAD + 64) * CV_KB;
constexpr int K1_LDS = 81920;

__global__ __launch_bounds__(256, 2) NOPK_ATTR void k1_gemm1_conv(
    const u16* __restrict__ xn, const u16* __restrict__ xnT, const u16* __restrict__ kexp,
    const float* __restrict__ kinvp, const u16* __restrict__ w1, const u16* __restrict__ b1, const u16* __restrict__ dskip,
    u16* __restrict__ hbuf, u16* __restrict__ sT, const u16* __restrict__ w2) {
  extern __shared__ __attribute__((aligned(16))) char smem[];
  const int tid = TIDX, lane = tid & 63, w = tid >> 6;
  if (BIDX < G1_BLOCKS) {
    const int bid = BIDX;
    const int xcd = bid & 7, rest = bid >> 3;
    const int mt_i = xcd * 2 + (rest & 1);
    const int nt_i = rest >> 1;
    const int m0 = mt_i * G1_BM, n0 = nt_i * G1_BN;
    const int fr = lane & 15, fq = lane >> 4;
    STAMPK(1, 0);
    const int wu = __builtin_amdgcn_readfirstlane(w);
    const char* gbase = (wu < 2) ? reinterpret_cast<const char*>(xn) : reinterpret_cast<const char*>(w1);
    const unsigned kstepb = (wu < 2) ? (unsigned)(G1_BK * 2) : (unsigned)(G1_BK * FF * 2);
    unsigned off[8];
#pragma unroll
    for (int i = 0; i < 8; ++i) {
      int p = (8 * w + i) * 64 + lane;
      if (wu < 2) {
        int row = p >> 3, ph = p & 7;
        off[i] = (unsigned)(((m0 + row) * DM + (ph ^ (row & 7)) * 8) * 2);
      } else {
        int c = p - 1024; if (c >= 896) c -= 128;
        int kr = c / 14, ch = c - kr * 14;
        off[i] = (unsigned)((kr * FF + n0 + ch * 8) * 2);
      }
    }
    auto issue = [&](int kt) {
      char* base = smem + (kt & 1) * G1_STAGE + (8 * w) * 1024;
      const char* g = gbase + (size_t)kt * kstepb;
#pragma unroll
      for (int i = 0; i < 8; ++i)
        __builtin_amdgcn_global_load_lds((const unsigned*)(g + off[i]),
                                         (unsigned*)(base + i * 1024 + lane * 16), 16, 0, 0);
    };
    u32x2 bbp[7];
#pragma unroll
    for (int j = 0; j < 7; ++j) bbp[j] = *reinterpret_cast<const u32x2*>(b1 + n0 + j * 16 + fq * 4);
    f32x4 acc[2][7];
#pragma unroll
    for (int i = 0; i < 2; ++i)
#pragma unroll
      for (int j = 0; j < 7; ++j) acc[i][j] = (f32x4){0.f, 0.f, 0.f, 0.f};
    constexpr int NKT = DM / G1_BK;
    issue(0);
    issue(1);
    const int q4 = (lane & 15) >> 2, p4 = lane & 3;
    bf16x4 tl[2][2][7], th[2][2][7];
    bf16x8 af[2][2][2];
    auto rdfrag = [&](auto ST) {
      constexpr int st = decltype(ST)::value;
      const char* A = smem + st * G1_STAGE;
      const unsigned baddr = lds_addr(smem) + st * G1_STAGE + G1_ABYTES + (fq * 4 + q4) * G1_BRS + p4 * 8;
      auto rd = [&](auto J) {
        constexpr int j = decltype(J)::value;
        tl[st][0][j] = tr_read<j * 32>(baddr);
        th[st][0][j] = tr_read<j * 32 + 16 * G1_BRS>(baddr);
        tl[st][1][j] = tr_read<j * 32 + 32 * G1_BRS>(baddr);
        th[st][1][j] = tr_read<j * 32 + 48 * G1_BRS>(baddr);
      };
      Unroll<0, 7>::run(rd);
#pragma unroll
      for (int ks = 0; ks < 2; ++ks)
#pragma unroll
        for (int i = 0; i < 2; ++i) {
          int row = w * 32 + i * 16 + fr;
          int c0 = ks * 4 + (fq >> 1), c1 = c0 + 2;
          bf16x4 x0 = *reinterpret_cast<const bf16x4*>(A + row * 128 + ((c0 ^ (row & 7)) << 4) + (fq & 1) * 8);
          bf16x4 x1 = *reinterpret_cast<const bf16x4*>(A + row * 128 + ((c1 ^ (row & 7)) << 4) + (fq & 1) * 8);
          af[st][ks][i] = __builtin_shufflevector(x0, x1, 0, 1, 2, 3, 4, 5, 6, 7);
        }
    };
    auto waitfrag = [&](auto ST) {
      constexpr int st = decltype(ST)::value;
      wait14(tl[st][0], th[st][0]);
      wait14(tl[st][1], th[st][1]);
    };
    auto mma = [&](auto ST) {
      constexpr int st = decltype(ST)::value;
#pragma unroll
      for (int ks = 0; ks < 2; ++ks)
#pragma unroll
        for (int j = 0; j < 7; ++j) {
          bf16x8 bfr = __builtin_shufflevector(tl[st][ks][j], th[st][ks][j], 0, 1, 2, 3, 4, 5, 6, 7);
#pragma unroll
          for (int i = 0; i < 2; ++i)
            acc[i][j] = __builtin_amdgcn_mfma_f32_16x16x32_bf16(bfr, af[st][ks][i], acc[i][j], 0, 0, 0);
        }
    };
    auto step = [&](auto ST, int kt) {
      constexpr int st = decltype(ST)::value;
      wait_vm<0>();
      __builtin_amdgcn_s_barrier();
      if (kt + 2 < NKT) issue(kt + 2);
      rdfrag(std::integral_constant<int, st ^ 1>{});
      mma(ST);
      waitfrag(std::integral_constant<int, st ^ 1>{});
    };
    wait_vm<8>();
    __builtin_amdgcn_s_barrier();
    rdfrag(std::integral_constant<int, 0>{});
    waitfrag(std::integral_constant<int, 0>{});
#pragma unroll 1
    for (int kt = 0; kt < 6; kt += 2) {
      step(std::integral_constant<int, 0>{}, kt);
      step(std::integral_constant<int, 1>{}, kt + 1);
    }
    mma(std::integral_constant<int, 0>{});
    STAMPK(1, 4);
    unsigned warm0, warm1 = 0;
    {
      const int o = (bid >> 3) * 50176 + tid * 128;
      warm0 = *reinterpret_cast<const unsigned*>(reinterpret_cast<const char*>(w2) + o);
      if (tid < 136) warm1 = *reinterpret_cast<const unsigned*>(reinterpret_cast<const char*>(w2) + o + 32768);
    }
    __builtin_amdgcn_sched_barrier(0);
    u32x2 pk[2][7];
#pragma unroll
    for (int i = 0; i < 2; ++i) {
#pragma unroll
      for (int j = 0; j < 7; ++j) {
#pragma unroll
        for (int r = 0; r < 2; ++r) {
          unsigned p0 = pk2bf(acc[i][j][2 * r], acc[i][j][2 * r + 1]);
          unsigned p1 = pk2bf(lo_f(p0) + lo_f(bbp[j][r]), hi_f(p0) + hi_f(bbp[j][r]));
          float t0 = __builtin_fmaxf(lo_f(p1), 0.f), t1 = __builtin_fmaxf(hi_f(p1), 0.f);
          pk[i][j][r] = pk2bf(t0 * t0, t1 * t1);
        }
      }
    }
#pragma unroll
    for (int i = 0; i < 2; ++i) {
      u16* hrow = hbuf + (size_t)(m0 + w * 32 + i * 16 + fr) * HS + n0;
#pragma unroll
      for (int P = 0; P < 3; ++P)
        *reinterpret_cast<u32x4*>(hrow + P * 32 + (fq & 1) * 16 + (fq >> 1) * 8) = pair_cols(pk[i][2 * P], pk[i][2 * P + 1]);
      *reinterpret_cast<u32x2*>(hrow + 96 + fq * 4) = pk[i][6];
    }
    asm volatile("" ::"v"(warm0), "v"(warm1));
    STAMPK(1, 5);
    return;
  }
  const int cb = BIDX - G1_BLOCKS;
  const int nch = (cb + 256 < DM) ? 2 : 1;
  char* XS = smem;
  char* KM = smem + CV_XBYTES;
  STAMPK(1, 0);
  u32x4 kv, cur[2], prv[2], xv;
  float dsk, kinv;
  auto prefetch = [&](int d) {
    const u16* xrow = xnT + (size_t)d * SL;
    kv = *reinterpret_cast<const u32x4*>(kexp + (size_t)d * SL + tid * 8);
#pragma unroll
    for (int rep = 0; rep < 2; ++rep) {
      int gg = tid + rep * 256 - 3;
      cur[rep] = (u32x4){0u, 0u, 0u, 0u}; prv[rep] = (u32x4){0u, 0u, 0u, 0u};
      if (rep == 0 || tid < 4) {
        if (gg >= 0 && gg < 256) cur[rep] = *reinterpret_cast<const u32x4*>(xrow + gg * 8);
        if (gg >= 1 && gg <= 256) prv[rep] = *reinterpret_cast<const u32x4*>(xrow + (gg - 1) * 8);
      }
    }
    xv = *reinterpret_cast<const u32x4*>(xrow + tid * 8);
    dsk = bf2f(dskip[d]);
    kinv = kinvp[d];
  };
  prefetch(cb);
  for (int i = tid; i < CV_KPAD * CV_KB / 16; i += 256)
    *reinterpret_cast<u32x4*>(KM + i * 16) = (u32x4){0u, 0u, 0u, 0u};
  for (int ch = 0; ch < nch; ++ch) {
    const int d = cb + ch * 256;
#pragma unroll
    for (int rep = 0; rep < 2; ++rep) {
      if (rep == 0 || tid < 4) {
        int gg = tid + rep * 256 - 3;
        int q = 256 - gg;
        unsigned Dw[8];
#pragma unroll
        for (int k = 0; k < 4; ++k) {
          Dw[k] = pk2h(lo_f(prv[rep][k]), hi_f(prv[rep][k]));
          Dw[4 + k] = pk2h(lo_f(cur[rep][k]), hi_f(cur[rep][k]));
        }
#pragma unroll
        for (int c = 0; c < 8; ++c) {
          u32x4 o;
#pragma unroll
          for (int k = 0; k < 4; ++k) {
            const int la = 8 + c - 2 * k;
            unsigned val;
            if (la & 1) {
              unsigned dd = Dw[(la - 1) >> 1];
              val = (dd >> 16) | (dd << 16);
            } else {
              val = (Dw[la >> 1] & 0xffffu) | (Dw[(la >> 1) - 1] & 0xffff0000u);
            }
            o[k] = val;
          }
          *reinterpret_cast<u32x4*>(XS + c * CV_XS + q * 16) = o;
        }
      }
    }
    {
      const int l0 = tid * 8;
      *reinterpret_cast<u32x4*>(KM + (CV_KPAD + (l0 >> 5)) * CV_KB + (l0 & 31) * 2) = kv;
    }
    const u32x4 xv_c = xv;
    const float dsk_c = dsk, kinv_c = kinv;
    bsync();
    if (ch == 0) STAMPK(1, 2);
    if (ch + 1 < nch) prefetch(d + 256);
    f32x16 acc0, acc1;
#pragma unroll
    for (int i = 0; i < 16; ++i) { acc0[i] = 0.f; acc1[i] = 0.f; }
    {
      const int t0 = lane & 31, hh = lane >> 5;
      const int c = t0 & 7;
      const char* abase = XS + c * CV_XS + 2 * (2048 - 8 * (t0 >> 3) + 8 * hh);
      const char* bbase = KM + (CV_KPAD + t0) * CV_KB + 16 * hh;
      f16x8 fa0[2][2], fa1[2][2], fb0[2][2], fb1[2][2];
      auto ldf = [&](int set, int it) {
        const int dl = w + 4 * it;
#pragma unroll
        for (int ks = 0; ks < 2; ++ks) {
          fa0[set][ks] = *reinterpret_cast<const f16x8*>(abase - 64 * dl + 32 * ks);
          fa1[set][ks] = *reinterpret_cast<const f16x8*>(abase - 64 * (dl + 32) + 32 * ks);
          const char* b0p = bbase - dl * CV_KB + 32 * ks;
          fb0[set][ks] = *reinterpret_cast<const f16x8*>(b0p);
          fb1[set][ks] = *reinterpret_cast<const f16x8*>(b0p + 32 * CV_KB);
        }
      };
      auto mm = [&](int set) {
#pragma unroll
        for (int ks = 0; ks < 2; ++ks) {
          acc0 = __builtin_amdgcn_mfma_f32_32x32x16_f16(fa0[set][ks], fb0[set][ks], acc0, 0, 0, 0);
          acc1 = __builtin_amdgcn_mfma_f32_32x32x16_f16(fa0[set][ks], fb1[set][ks], acc1, 0, 0, 0);
          acc1 = __builtin_amdgcn_mfma_f32_32x32x16_f16(fa1[set][ks], fb0[set][ks], acc1, 0, 0, 0);
        }
      };
      ldf(0, 0);
#pragma unroll 1
      for (int it = 0; it < 6; it += 2) {
        ldf(1, it + 1);
        __builtin_amdgcn_sched_barrier(0);
        mm(0);
        __builtin_amdgcn_sched_barrier(0);
        ldf(0, it + 2);
        __builtin_amdgcn_sched_barrier(0);
        mm(1);
        __builtin_amdgcn_sched_barrier(0);
      }
      ldf(1, 7);
      __builtin_amdgcn_sched_barrier(0);
      mm(0);
      mm(1);
    }
    bsync();
    if (ch == 0) STAMPK(1, 3);
    float* red = reinterpret_cast<float*>(smem);
    {
      const int col = lane & 31, hh = lane >> 5;
#pragma unroll
      for (int g = 0; g < 4; ++g) {
        f32x4 v0 = (f32x4){acc0[4 * g], acc0[4 * g + 1], acc0[4 * g + 2], acc0[4 * g + 3]};
        f32x4 v1 = (f32x4){acc1[4 * g], acc1[4 * g + 1], acc1[4 * g + 2], acc1[4 * g + 3]};
        *reinterpret_cast<f32x4*>(red + w * 2304 + 36 * col + 8 * g + 4 * hh) = v0;
        *reinterpret_cast<f32x4*>(red + w * 2304 + 36 * (32 + col) + 8 * g + 4 * hh) = v1;
      }
    }
    bsync();
    {
      const int t = tid * 8;
      f32x4 y0 = (f32x4){0.f, 0.f, 0.f, 0.f}, y1 = (f32x4){0.f, 0.f, 0.f, 0.f};
#pragma unroll
      for (int ww = 0; ww < 4; ++ww) {
        y0 += *reinterpret_cast<const f32x4*>(red + ww * 2304 + 36 * (tid >> 2) + 8 * (tid & 3));
        y1 += *reinterpret_cast<const f32x4*>(red + ww * 2304 + 36 * (tid >> 2) + 8 * (tid & 3) + 4);
      }
      u32x4 o;
#pragma unroll
      for (int j = 0; j < 4; ++j) {
        float ya = (j < 2 ? y0[2 * j] : y1[2 * j - 4]) * kinv_c;
        float yb = (j < 2 ? y0[2 * j + 1] : y1[2 * j - 3]) * kinv_c;
        o[j] = pk2bf(rbf(ya) + rbf(lo_f(xv_c[j]) * dsk_c), rbf(yb) + rbf(hi_f(xv_c[j]) * dsk_c));
      }
      *reinterpret_cast<u32x4*>(sT + (size_t)d * SL + t) = o;
    }
    if (ch == 0) STAMPK(1, 4);
    if (ch + 1 < nch) bsync();
  }

  STAMPK(1, 5);
}

constexpr int G2_BM = 64, G2_BN = 64, G2_BK = 128;
constexpr int G2_BLOCKS = (SL / G2_BM) * (DM / G2_BN);
constexpr int G2_ABYTES = G2_BM * G2_BK * 2;
constexpr int G2_STAGE = 32768;
#define G2NS 4
constexpr int G2_NS = G2NS;
constexpr int K2_LDS = G2_NS * G2_STAGE;

__device__ __forceinline__ void wait8(bf16x4 (&a)[2][2][2]) {
  asm volatile("s_waitcnt lgkmcnt(0)"
               : "+v"(a[0][0][0]), "+v"(a[0][0][1]), "+v"(a[0][1][0]), "+v"(a[0][1][1]),
                 "+v"(a[1][0][0]), "+v"(a[1][0][1]), "+v"(a[1][1][0]), "+v"(a[1][1][1]));
}
__device__ __forceinline__ int g2_bswz(int r) { return ((r >> 1) & 3) << 1; }

__global__ __launch_bounds__(256, 1) NOPK_ATTR void k2_gemm2(
    const u16* __restrict__ hbuf, const u16* __restrict__ w2, const u16* __restrict__ b2,
    const u16* __restrict__ u, const u16* __restrict__ sT, u16* __restrict__ out) {
  extern __shared__ __attribute__((aligned(16))) char smem[];
  const int tid = TIDX, lane = tid & 63, w = tid >> 6;
  const int bid = BIDX;
  const int xcd = bid & 7, rest = bid >> 3;
  const int mt_i = xcd * 4 + (rest & 3);
  const int nt_i = rest >> 2;
  const int m0 = mt_i * G2_BM, n0 = nt_i * G2_BN;
  const int fr = lane & 15, fq = lane >> 4;
  const int wm = w & 1, wn = w >> 1;
  STAMPK(2, 0);
  const int ncol = wn * 32 + (fq & 1) * 16 + (fq >> 1) * 8;
  u32x4 e_u[2];
  u16 e_s[2][8];
#pragma unroll
  for (int i = 0; i < 2; ++i) {
    const int m = m0 + wm * 32 + i * 16 + fr, n = n0 + ncol;
    e_u[i] = *reinterpret_cast<const u32x4*>(u + (size_t)m * DM + n);
#pragma unroll
    for (int r = 0; r < 8; ++r) e_s[i][r] = sT[(size_t)(n + r) * SL + m];
  }
  float bb[2][4];
#pragma unroll
  for (int j = 0; j < 2; ++j) {
    u32x2 t = *reinterpret_cast<const u32x2*>(b2 + n0 + wn * 32 + j * 16 + fq * 4);
    bb[j][0] = lo_f(t[0]); bb[j][1] = hi_f(t[0]); bb[j][2] = lo_f(t[1]); bb[j][3] = hi_f(t[1]);
  }
  const u16* srcA[2];
  const u16* srcB[2];
#pragma unroll
  for (int i = 0; i < 2; ++i) {
    int p = (2 * w + i) * 64 + lane;
    int row = p >> 3, ph = p & 7;
    srcA[i] = hbuf + (size_t)(m0 + row) * HS + (ph ^ (row & 7)) * 8;
    srcB[i] = w2 + (size_t)row * DM + n0 + (ph ^ g2_bswz(row)) * 8;
  }
  auto issue = [&](int kt) {
    char* base = smem + (kt % G2_NS) * G2_STAGE + (2 * w) * 1024 + lane * 16;
#pragma unroll
    for (int hf = 0; hf < 2; ++hf)
#pragma unroll
      for (int i = 0; i < 2; ++i) {
        __builtin_amdgcn_global_load_lds((const unsigned*)(srcA[i] + kt * G2_BK + hf * 64),
                                         (unsigned*)(base + hf * 8192 + i * 1024), 16, 0, 0);
        __builtin_amdgcn_global_load_lds((const unsigned*)(srcB[i] + (size_t)(kt * G2_BK + hf * 64) * DM),
                                         (unsigned*)(base + G2_ABYTES + hf * 8192 + i * 1024), 16, 0, 0);
      }
  };
  f32x4 acc[2][2];
#pragma unroll
  for (int i = 0; i < 2; ++i)
#pragma unroll
    for (int j = 0; j < 2; ++j) acc[i][j] = (f32x4){0.f, 0.f, 0.f, 0.f};
  constexpr int NKT = FF / G2_BK;
#pragma unroll
  for (int s0 = 0; s0 < G2_NS - 1; ++s0) issue(s0);
  const int q4 = (lane & 15) >> 2, p4 = lane & 3;
  bf16x4 t[2][2][2][2][2];
  bf16x4 xa[2][2][2][2][2];
  auto rdfrag = [&](auto KT) {
    constexpr int kt = decltype(KT)::value;
    constexpr int st = kt & 1;
    const char* A = smem + (kt % G2_NS) * G2_STAGE;
    const unsigned bbase = lds_addr(smem) + (kt % G2_NS) * G2_STAGE + G2_ABYTES;
#pragma unroll
    for (int j = 0; j < 2; ++j)
#pragma unroll
      for (int hf = 0; hf < 2; ++hf) {
        const int r0 = fq * 4 + q4 + 16 * hf;
        const int ch = (wn * 2 + j) * 2 + (p4 >> 1);
        const unsigned a0 = bbase + r0 * 128 + ((ch ^ g2_bswz(r0)) << 4) + (p4 & 1) * 8;
        t[st][0][0][j][hf] = tr_read<0>(a0);
        t[st][0][1][j][hf] = tr_read<32 * 128>(a0);
        t[st][1][0][j][hf] = tr_read<64 * 128>(a0);
        t[st][1][1][j][hf] = tr_read<96 * 128>(a0);
      }
#pragma unroll
    for (int ks = 0; ks < 2; ++ks)
#pragma unroll
      for (int i = 0; i < 2; ++i)
#pragma unroll
        for (int hf = 0; hf < 2; ++hf) {
          int row = wm * 32 + i * 16 + fr;
          int c0 = ks * 4 + (fq >> 1) + 2 * hf;
          const unsigned a0 = lds_addr(A) + row * 128 + ((c0 ^ (row & 7)) << 4) + (fq & 1) * 8;
          xa[st][0][ks][i][hf] = ld_b64<0>(a0);
          xa[st][1][ks][i][hf] = ld_b64<8192>(a0);
        }
  };
  auto waitfrag = [&](auto ST) {
    constexpr int st = decltype(ST)::value;
    wait8(t[st][0]);
    wait8(t[st][1]);
    wait8(xa[st][0]);
    wait8(xa[st][1]);
  };
  wait_vm<8 * (G2_NS - 2)>();
  __builtin_amdgcn_s_barrier();
  STAMPK(2, 1);
  rdfrag(std::integral_constant<int, 0>{});
  waitfrag(std::integral_constant<int, 0>{});
  auto step = [&](auto KT) {
    constexpr int kt = decltype(KT)::value;
    constexpr int st = kt & 1;
    if constexpr (kt + 1 < NKT) {
      constexpr int last = (kt + G2_NS - 2) < (NKT - 1) ? (kt + G2_NS - 2) : (NKT - 1);
      wait_vm<8 * (last - (kt + 1))>();
      __builtin_amdgcn_s_barrier();
      if constexpr (kt == 3) STAMPK(2, 2);
      if constexpr (kt == 7) STAMPK(2, 3);
      if constexpr (kt == 11) STAMPK(2, 4);
      if constexpr (kt + G2_NS - 1 < NKT) issue(kt + G2_NS - 1);
      rdfrag(std::integral_constant<int, kt + 1>{});
    }
#pragma unroll
    for (int hv = 0; hv < 2; ++hv)
#pragma unroll
      for (int ks = 0; ks < 2; ++ks)
#pragma unroll
        for (int j = 0; j < 2; ++j) {
          bf16x8 bfr = __builtin_shufflevector(t[st][hv][ks][j][0], t[st][hv][ks][j][1], 0, 1, 2, 3, 4, 5, 6, 7);
#pragma unroll
          for (int i = 0; i < 2; ++i)
            acc[i][j] = __builtin_amdgcn_mfma_f32_16x16x32_bf16(
                bfr, __builtin_shufflevector(xa[st][hv][ks][i][0], xa[st][hv][ks][i][1], 0, 1, 2, 3, 4, 5, 6, 7),
                acc[i][j], 0, 0, 0);
        }
    if constexpr (kt + 1 < NKT) waitfrag(std::integral_constant<int, st ^ 1>{});
  };
  Unroll<0, NKT>::run(step);
  STAMPK(2, 5);
#pragma unroll
  for (int i = 0; i < 2; ++i) {
    const int m = m0 + wm * 32 + i * 16 + fr, n = n0 + ncol;
    u32x2 gp[2];
#pragma unroll
    for (int j = 0; j < 2; ++j) {
#pragma unroll
      for (int r = 0; r < 2; ++r) {
        unsigned p0 = pk2bf(acc[i][j][2 * r], acc[i][j][2 * r + 1]);
        unsigned p1 = pk2bf(lo_f(p0) + bb[j][2 * r], hi_f(p0) + bb[j][2 * r + 1]);
        float z0 = lo_f(p1), z1 = hi_f(p1);
        gp[j][r] = pk2bf(silu_f(z0), silu_f(z1));
      }
    }
    const u32x4 gv = pair_cols(gp[0], gp[1]);
    u32x4 o;
#pragma unroll
    for (int e = 0; e < 4; ++e)
      o[e] = pk2bf(lo_f(e_u[i][e]) + rbf(bf2f(e_s[i][2 * e]) * lo_f(gv[e])),
                   hi_f(e_u[i][e]) + rbf(bf2f(e_s[i][2 * e + 1]) * hi_f(gv[e])));
    *reinterpret_cast<u32x4*>(out + (size_t)m * DM + n) = o;
  }
  STAMPK(2, 6);
}

#if defined(__HIP_DEVICE_COMPILE__)
#pragma clang attribute pop
#endif
extern "C" void kernel_launch(void* const* d_in, const int* in_sizes, int n_in,
                              void* d_out, int out_size, void* d_ws, size_t ws_size,
                              hipStream_t stream) {
  const u16* u = (const u16*)d_in[0];
  const u16* logA = (const u16*)d_in[1];
  const u16* Aim = (const u16*)d_in[2];
  const u16* Cre = (const u16*)d_in[3];
  const u16* Cim = (const u16*)d_in[4];
  const u16* logdt = (const u16*)d_in[5];
  const u16* dskip = (const u16*)d_in[6];
  const u16* w1 = (const u16*)d_in[8];
  const u16* b1 = (const u16*)d_in[9];
  const u16* w2 = (const u16*)d_in[10];
  const u16* b2 = (const u16*)d_in[11];
  u16* out = (u16*)d_out;
  char* ws = (char*)d_ws;
  u16* xn = (u16*)(ws);
  u16* xnT = (u16*)(ws + (2u << 20));
  u16* sT = (u16*)(ws + (4u << 20));
  u16* kexp = (u16*)(ws + (6u << 20));
  float* kinvp = (float*)(ws + (7u << 20) + (900u << 10));
  u16* hbuf = (u16*)(ws + (8u << 20));

  hipFuncSetAttribute((const void*)k0_ln_kgen, hipFuncAttributeMaxDynamicSharedMemorySize, K0_LDS);
  hipFuncSetAttribute((const void*)k1_gemm1_conv, hipFuncAttributeMaxDynamicSharedMemorySize, K1_LDS);
  hipFuncSetAttribute((const void*)k2_gemm2, hipFuncAttributeMaxDynamicSharedMemorySize, K2_LDS);
  k0_ln_kgen<<<LN_BLOCKS + DM, 256, K0_LDS, stream>>>(u, logA, Aim, Cre, Cim, logdt, xn, xnT, kexp, kinvp, w1, (const char*)ws, (const char*)out, (const char*)w2);
  k1_gemm1_conv<<<G1_BLOCKS + 256, 256, K1_LDS, stream>>>(xn, xnT, kexp, kinvp, w1, b1, dskip, hbuf, sT, w2);
  k2_gemm2<<<G2_BLOCKS, 256, K2_LDS, stream>>>(hbuf, w2, b2, u, sT, out);
}
```

```cpp
#include <hip/hip_runtime.h>
#include <hip/hip_bf16.h>
#include <stdint.h>
#include <type_traits>

#if defined(__HIP_DEVICE_COMPILE__)
#pragma clang attribute push(__attribute__((target("no-packed-fp32-ops"))), apply_to = function)
#endif
typedef unsigned short u16;
typedef __attribute__((ext_vector_type(8))) short bf16x8;
typedef __attribute__((ext_vector_type(4))) short bf16x4;
typedef __attribute__((ext_vector_type(8))) _Float16 f16x8;
typedef __attribute__((ext_vector_type(2))) _Float16 f16x2;
typedef __attribute__((ext_vector_type(2))) __bf16 bf16x2;
typedef __attribute__((ext_vector_type(2))) float f32x2;
typedef __attribute__((ext_vector_type(4))) float f32x4;
typedef __attribute__((ext_vector_type(16))) float f32x16;
typedef __attribute__((ext_vector_type(4))) unsigned int u32x4;
typedef __attribute__((ext_vector_type(2))) unsigned int u32x2;
#define LDS_AS __attribute__((address_space(3)))

#define TIDX ((int)__builtin_amdgcn_workitem_id_x())
#define BIDX ((int)__builtin_amdgcn_workgroup_id_x())
__device__ __forceinline__ float u2f(unsigned v) { return __builtin_bit_cast(float, v); }
__device__ __forceinline__ unsigned f2u(float v) { return __builtin_bit_cast(unsigned, v); }
__device__ __forceinline__ void bsync() {
  __builtin_amdgcn_fence(__ATOMIC_RELEASE, "workgroup");
  __builtin_amdgcn_s_barrier();
  __builtin_amdgcn_fence(__ATOMIC_ACQUIRE, "workgroup");
}
__device__ __forceinline__ float shfl_xor_f(float v, int m, int lane) {
  return u2f((unsigned)__builtin_amdgcn_ds_bpermute((lane ^ m) << 2, (int)f2u(v)));
}
__device__ __forceinline__ float exp_acc(float x) {
  const float L2E_HI = 1.44269502162933349609375f, L2E_LO = 1.92596299112661746e-8f;
  float t = x * L2E_HI;
  float e = __builtin_fmaf(x, L2E_HI, -t) + x * L2E_LO;
  float r = __builtin_amdgcn_exp2f(t);
  return __builtin_fmaf(r, e * 0.693147180559945309f, r);
}

__device__ __forceinline__ float silu_f(float z) {
  float dn = 1.0f + exp_acc(-z);
  float r = __builtin_amdgcn_rcpf(dn);
  float q = z * r;
  float e = __builtin_fmaf(-q, dn, z);
  float res = __builtin_fmaf(e, r, q);
  return (dn < 3.0e38f) ? res : -0.0f * 0.f + (z * 0.f);
}

constexpr int SL = 2048;
constexpr int DM = 448;
constexpr int NMODE = 256;
constexpr int FF = 1792;
#define HPAD 64
constexpr int HS = FF + HPAD;
constexpr int KTAB = 48 * 16;

__device__ __forceinline__ float bf2f(u16 v) { return u2f(((unsigned)v) << 16); }
__device__ __forceinline__ unsigned pk2bf(float a, float b) {
  f32x2 v = {a, b};
  bf16x2 r = __builtin_convertvector(v, bf16x2);
  return *reinterpret_cast<unsigned*>(&r);
}
__device__ __forceinline__ u16 f2bf(float f) { return (u16)(pk2bf(f, 0.f) & 0xffffu); }
__device__ __forceinline__ float rbf(float f) { return u2f(pk2bf(f, 0.f) << 16); }
__device__ __forceinline__ float lo_f(unsigned v) { return u2f(v << 16); }
__device__ __forceinline__ float hi_f(unsigned v) { return u2f(v & 0xffff0000u); }
__device__ __forceinline__ unsigned pk2h(float a, float b) {
  f32x2 v = {a, b};
  f16x2 r = __builtin_convertvector(v, f16x2);
  return *reinterpret_cast<unsigned*>(&r);
}

template <int I, int N> struct Unroll {
  template <class F> static __device__ __forceinline__ void run(F& f) {
    f(std::integral_constant<int, I>{});
    Unroll<I + 1, N>::run(f);
  }
};
template <int N> struct Unroll<N, N> {
  template <class F> static __device__ __forceinline__ void run(F&) {}
};
template <int N> __device__ __forceinline__ void wait_vm() {
  asm volatile("s_waitcnt vmcnt(%0)" ::"n"(N) : "memory");
}
__device__ __forceinline__ void wait_lgkm0() { asm volatile("s_waitcnt lgkmcnt(0)" ::: "memory"); }

template <int OFF> __device__ __forceinline__ bf16x4 tr_read(unsigned addr) {
  bf16x4 r;
  asm volatile("ds_read_b64_tr_b16 %0, %1 offset:%2" : "=v"(r) : "v"(addr), "n"(OFF));
  return r;
}
__device__ __forceinline__ void wait14(bf16x4 (&a)[7], bf16x4 (&b)[7]) {
  asm volatile("s_waitcnt lgkmcnt(0)"
               : "+v"(a[0]), "+v"(a[1]), "+v"(a[2]), "+v"(a[3]), "+v"(a[4]), "+v"(a[5]), "+v"(a[6]),
                 "+v"(b[0]), "+v"(b[1]), "+v"(b[2]), "+v"(b[3]), "+v"(b[4]), "+v"(b[5]), "+v"(b[6]));
}
template <int OFF> __device__ __forceinline__ bf16x4 ld_b64(unsigned addr) {
  bf16x4 r;
  asm volatile("ds_read_b64 %0, %1 offset:%2" : "=v"(r) : "v"(addr), "n"(OFF));
  return r;
}
__device__ __forceinline__ unsigned lds_addr(const void* p) { return (unsigned)(size_t)p; }

#define STAMPK(k, s)
__device__ __forceinline__ u32x4 pair_cols(u32x2 a, u32x2 b) {
  auto r0 = __builtin_amdgcn_permlane16_swap(a[0], b[0], false, false);
  auto r1 = __builtin_amdgcn_permlane16_swap(a[1], b[1], false, false);
  return (u32x4){r0[0], r1[0], r0[1], r1[1]};
}
#define CODE_ADDR(var, sym)                                                                   \
  asm volatile("s_getpc_b64 s[20:21]\n\t"                                                     \
               "s_add_u32 s20, s20, " sym "@rel32@lo+4\n\t"                                   \
               "s_addc_u32 s21, s21, " sym "@rel32@hi+12\n\t"                                 \
               "s_mov_b64 %0, s[20:21]"                                                       \
               : "=s"(var) : : "s20", "s21", "scc")
struct cf { float r, i; };
__device__ __forceinline__ cf cmul(cf a, cf b) {
  cf o; o.r = a.r * b.r - a.i * b.i; o.i = a.r * b.i + a.i * b.r; return o;
}
__device__ __forceinline__ cf cexp_p(float ar, float ai, float p) {
  float m = __builtin_amdgcn_exp2f((ar * p) * 1.4426950408889634f);
  float rev = (ai * p) * 0.15915494309189535f;
  rev = __builtin_amdgcn_fractf(rev);
  cf o; o.r = m * __builtin_amdgcn_cosf(rev); o.i = m * __builtin_amdgcn_sinf(rev); return o;
}
template <int CTRL> __device__ __forceinline__ float dpp_add(float s) {
  int t = __builtin_amdgcn_update_dpp(0, (int)f2u(s), CTRL, 0xf, 0xf, true);
  return s + u2f((unsigned)t);
}
__device__ __forceinline__ float red16(float s) {
  s = dpp_add<0xB1>(s);
  s = dpp_add<0x4E>(s);
  s = dpp_add<0x141>(s);
  s = dpp_add<0x140>(s);
  return s;
}

constexpr int LN_ROWS = 16;
constexpr int LN_BLOCKS = SL / LN_ROWS;
constexpr int KG_RS = 136;
constexpr int K0_LDS = 4 * 2 * 16 * KG_RS * 2 + 64;

__device__ __forceinline__ int ktab_index(int l) {
  int pos, r, b;
  if (l < 256) { pos = l; r = ((pos >> 7) << 3) + (pos & 7); b = (pos >> 3) & 15; return r * 16 + b; }
  pos = (int)rbf((float)l);
  if (pos < 512) { int q = pos - 256; r = 16 + ((q >> 7) << 2) + ((q >> 1) & 3); b = (q >> 3) & 15; }
  else if (pos < 1024) { int q = pos - 512; r = 24 + ((q >> 7) << 1) + ((q >> 2) & 1); b = (q >> 3) & 15; }
  else if (pos < 2048) { int q = pos - 1024; r = 32 + (q >> 7); b = (q >> 3) & 15; }
  else { r = 40; b = 0; }
  return r * 16 + b;
}

#define NOPK_ATTR
__global__ __launch_bounds__(256) NOPK_ATTR void k0_ln_kgen(
    const u16* __restrict__ u, const u16* __restrict__ logA, const u16* __restrict__ Aim,
    const u16* __restrict__ Cre, const u16* __restrict__ Cim, const u16* __restrict__ logdt,
    u16* __restrict__ xn, u16* __restrict__ xnT, u16* __restrict__ kexp, float* __restrict__ kinvp,
    const u16* __restrict__ w1) {
  extern __shared__ __attribute__((aligned(16))) char smem[];
  const int tid = TIDX, lane = tid & 63, w = tid >> 6;
  if (BIDX < LN_BLOCKS) {
    u16* tile = reinterpret_cast<u16*>(smem);
    constexpr int TS = DM + 8;
    STAMPK(0, 0);
    const int rb = tid >> 4, l16 = tid & 15;
    const int rblk = ((BIDX & 7) << 4) + (BIDX >> 3);
    const int row = rblk * LN_ROWS + rb;
    u32x2 v[7];
#pragma unroll
    for (int i = 0; i < 7; ++i)
      v[i] = *reinterpret_cast<const u32x2*>(u + (size_t)row * DM + i * 64 + l16 * 4);
    float x[28];
#pragma unroll
    for (int i = 0; i < 7; ++i) {
      x[4 * i] = lo_f(v[i][0]); x[4 * i + 1] = hi_f(v[i][0]);
      x[4 * i + 2] = lo_f(v[i][1]); x[4 * i + 3] = hi_f(v[i][1]);
    }
    float s = 0.f;
#pragma unroll
    for (int j = 0; j < 28; ++j) s += x[j];
    s = red16(s);
    const float mean = s * (1.0f / DM);
    float q = 0.f;
#pragma unroll
    for (int j = 0; j < 28; ++j) { float dd = x[j] - mean; q += dd * dd; }
    q = red16(q);
    const float var_b = rbf(q * (1.0f / DM));
    const float mean_b = rbf(mean);
    const float den = rbf(__builtin_sqrtf(rbf(var_b + 1e-5f)));
#pragma unroll
    for (int i = 0; i < 7; ++i) {
      u32x2 o;
      o[0] = pk2bf(rbf(x[4 * i] - mean_b) / den, rbf(x[4 * i + 1] - mean_b) / den);
      o[1] = pk2bf(rbf(x[4 * i + 2] - mean_b) / den, rbf(x[4 * i + 3] - mean_b) / den);
      *reinterpret_cast<u32x2*>(xn + (size_t)row * DM + i * 64 + l16 * 4) = o;
      *reinterpret_cast<u32x2*>(tile + rb * TS + i * 64 + l16 * 4) = o;
    }
    bsync();
    STAMPK(0, 1);
    for (int d = tid; d < DM; d += 256) {
      unsigned pk[8];
#pragma unroll
      for (int r = 0; r < 8; ++r)
        pk[r] = (unsigned)tile[(2 * r) * TS + d] | ((unsigned)tile[(2 * r + 1) * TS + d] << 16);
      u32x4* dst = reinterpret_cast<u32x4*>(xnT + (size_t)d * SL + rblk * LN_ROWS);
      dst[0] = (u32x4){pk[0], pk[1], pk[2], pk[3]};
      dst[1] = (u32x4){pk[4], pk[5], pk[6], pk[7]};
    }
    {
      unsigned wv[4];
      const int o = (BIDX >> 3) * 100352 + tid * 128;
#pragma unroll
      for (int q = 0; q < 4; ++q) {
        wv[q] = 0;
        if (q < 3 || tid < 16) wv[q] = *reinterpret_cast<const unsigned*>(reinterpret_cast<const char*>(w1) + o + q * 32768);
      }
      asm volatile("" ::"v"(wv[0]), "v"(wv[1]), "v"(wv[2]), "v"(wv[3]));
      unsigned long long ca, ce;
      CODE_ADDR(ca, "_Z13k1_gemm1_convPKtS0_S0_PKfS0_S0_S0_PtS3_S0_");
      CODE_ADDR(ce, "_Z8k2_gemm2PKtS0_S0_S0_S0_Pt");
      unsigned cv = 0;
      if (ce > ca && (unsigned long long)(tid * 64 + 4) <= ce - ca) cv = *reinterpret_cast<const unsigned*>(ca + tid * 64);
      asm volatile("" ::"v"(cv));
    }
    STAMPK(0, 5);
    return;
  }
  const int d = BIDX - LN_BLOCKS;
  u16* wbase = reinterpret_cast<u16*>(smem) + w * (2 * 16 * KG_RS);
  u16* Vb = wbase;
  u16* Wb = wbase + 16 * KG_RS;
  STAMPK(0, 0);
  const int idx = d * NMODE + tid;
  const u16 r_ldt = logdt[d], r_la = logA[idx], r_ai = Aim[idx], r_cr = Cre[idx], r_ci = Cim[idx];
  const float dt = rbf(exp_acc(bf2f(r_ldt)));
  const float Are = -rbf(exp_acc(bf2f(r_la)));
  const float ar = Are * dt;
  const float ai = bf2f(r_ai) * dt;
  cf C2; C2.r = 2.0f * (bf2f(r_cr) * dt); C2.i = 2.0f * (bf2f(r_ci) * dt);
  float* RM = reinterpret_cast<float*>(smem + 4 * 2 * 16 * KG_RS * 2);
  float wsc, wsc_inv;
  {
    float m = __builtin_fmaxf(__builtin_fabsf(C2.r), __builtin_fabsf(C2.i));
    float sm = __builtin_fabsf(C2.r) + __builtin_fabsf(C2.i);
#pragma unroll
    for (int o = 32; o > 0; o >>= 1) { m = __builtin_fmaxf(m, shfl_xor_f(m, o, lane)); sm += shfl_xor_f(sm, o, lane); }
    if (lane == 0) RM[w] = sm;
    int e = (int)((f2u(m) >> 23) & 0xff) - 127;
    if (m == 0.f || e > 100 || e < -100) e = 13;
    wsc = u2f((unsigned)(127 + 13 - e) << 23);
    wsc_inv = u2f((unsigned)(127 - 13 + e) << 23);
  }
  C2.r *= wsc; C2.i *= wsc;
  auto st = [&](u16* buf, int row, float re, float im) {
    *reinterpret_cast<unsigned*>(buf + row * KG_RS + 2 * lane) = pk2h(re, im);
  };
  {
    cf V1 = cexp_p(ar, ai, 8.f);
    cf V8 = cexp_p(ar, ai, 64.f);
    cf V[8];
    V[0].r = 1.f; V[0].i = 0.f;
    V[1] = V1;
    V[2] = cmul(V1, V1);
    V[3] = cmul(V[2], V1);
    V[4] = cmul(V[2], V[2]);
    V[5] = cmul(V[4], V1);
    V[6] = cmul(V[4], V[2]);
    V[7] = cmul(V[4], V[3]);
#pragma unroll
    for (int b = 0; b < 8; ++b) st(Vb, b, V[b].r, V[b].i);
    st(Vb, 8, V8.r, V8.i);
#pragma unroll
    for (int b = 1; b < 8; ++b) { cf t = cmul(V8, V[b]); st(Vb, 8 + b, t.r, t.i); }
  }
  cf U[8];
  {
    cf E1 = cexp_p(ar, ai, 1.f);
    cf E2 = cmul(E1, E1);
    cf E3 = cmul(E2, E1);
    cf E4 = cmul(E2, E2);
    U[0] = C2;
    U[1] = cmul(C2, E1);
    U[2] = cmul(C2, E2);
    U[3] = cmul(C2, E3);
    U[4] = cmul(C2, E4);
    U[5] = cmul(U[4], E1);
    U[6] = cmul(U[4], E2);
    U[7] = cmul(U[4], E3);
  }
  const cf F1 = cexp_p(ar, ai, 128.f);
  const cf F2 = cexp_p(ar, ai, 256.f);
  const cf F4 = cexp_p(ar, ai, 512.f);
  const cf F8 = cexp_p(ar, ai, 1024.f);
  const cf F3 = cmul(F2, F1);

  f32x4 acc[3];
#pragma unroll
  for (int mt = 0; mt < 3; ++mt) acc[mt] = (f32x4){0.f, 0.f, 0.f, 0.f};
  const int fr = lane & 15, fq = lane >> 4;
  asm volatile("" ::: "memory");
  f16x8 bv[4];
#pragma unroll
  for (int s4 = 0; s4 < 4; ++s4)
    bv[s4] = *reinterpret_cast<const f16x8*>(Vb + fr * KG_RS + 32 * s4 + 8 * fq);
  auto tile = [&](int mt, const cf* T) {
    asm volatile("" ::: "memory");
#pragma unroll
    for (int r = 0; r < 16; ++r) st(Wb, r, T[r].r, -T[r].i);
    asm volatile("" ::: "memory");
#pragma unroll
    for (int s4 = 0; s4 < 4; ++s4) {
      f16x8 a = *reinterpret_cast<const f16x8*>(Wb + fr * KG_RS + 32 * s4 + 8 * fq);
      acc[mt] = __builtin_amdgcn_mfma_f32_16x16x32_f16(a, bv[s4], acc[mt], 0, 0, 0);
    }
    asm volatile("" ::: "memory");
  };
  cf T[16];
#pragma unroll
  for (int e = 0; e < 8; ++e) { T[e] = U[e]; T[8 + e] = cmul(F1, U[e]); }
  STAMPK(0, 1);
  tile(0, T);
  STAMPK(0, 2);
  cf G[4];
  G[0] = F4; G[1] = cmul(F4, F1); G[2] = cmul(F4, F2); G[3] = cmul(F4, F3);
#pragma unroll
  for (int e = 0; e < 4; ++e) {
    T[e] = cmul(F2, U[2 * e]);
    T[4 + e] = cmul(F3, U[2 * e]);
    T[8 + 2 * e] = cmul(G[e], U[0]);
    T[8 + 2 * e + 1] = cmul(G[e], U[4]);
  }
  tile(1, T);
  {
    cf H0 = cmul(F8, U[0]);
    T[0] = H0; T[1] = cmul(H0, F1); T[2] = cmul(H0, F2); T[3] = cmul(H0, F3);
#pragma unroll
    for (int a2 = 0; a2 < 4; ++a2) T[4 + a2] = cmul(H0, G[a2]);
    T[8] = cmul(H0, F8);
#pragma unroll
    for (int r = 9; r < 16; ++r) { T[r].r = 0.f; T[r].i = 0.f; }
  }
  tile(2, T);
#pragma unroll
  for (int mt = 0; mt < 3; ++mt) acc[mt] *= wsc_inv;
  bsync();
  STAMPK(0, 3);
  float* red = reinterpret_cast<float*>(smem);
#pragma unroll
  for (int mt = 0; mt < 3; ++mt)
#pragma unroll
    for (int j = 0; j < 4; ++j) red[w * KTAB + (mt * 16 + fq * 4 + j) * 16 + fr] = acc[mt][j];
  bsync();
  float* KT = red + 4 * KTAB;
#pragma unroll
  for (int r = 0; r < 3; ++r) {
    int i = tid + r * 256;
    KT[i] = (red[i] + red[KTAB + i]) + (red[2 * KTAB + i] + red[3 * KTAB + i]);
  }
  bsync();
  {
    float m = (RM[0] + RM[1]) + (RM[2] + RM[3]);
    int e = (int)((f2u(m) >> 23) & 0xff) - 127;
    if (m == 0.f || e > 100 || e < -100) e = 14;
    const float kscale = u2f((unsigned)(127 + 14 - e) << 23);
    if (tid == 0) kinvp[d] = u2f((unsigned)(127 - 14 + e) << 23);
    const int l0 = tid * 8;
    u32x4 hv;
#pragma unroll
    for (int j = 0; j < 4; ++j) {
      float v0 = KT[ktab_index(l0 + 2 * j)], v1 = KT[ktab_index(l0 + 2 * j + 1)];
      hv[j] = pk2h(v0 * kscale, v1 * kscale);
    }
    *reinterpret_cast<u32x4*>(kexp + (size_t)d * SL + l0) = hv;
  }

  STAMPK(0, 5);
}

constexpr int G1_BM = 128, G1_BN = 112, G1_BK = 64;
constexpr int G1_BLOCKS = (SL / G1_BM) * (FF / G1_BN);
constexpr int G1_ABYTES = G1_BM * G1_BK * 2;
constexpr int G1_BRS = 224;
constexpr int G1_STAGE = 32768;
constexpr int G1_NS = 2;
constexpr int CV_XS = 4160;
constexpr int CV_XBYTES = 36864;
constexpr int CV_KB = 80;
constexpr int CV_KPAD = 31;
constexpr int CV_KBYTES = (CV_KPAD + 64) * CV_KB;
constexpr int K1_LDS = 81920;

__global__ __launch_bounds__(256, 2) NOPK_ATTR void k1_gemm1_conv(
    const u16* __restrict__ xn, const u16* __restrict__ xnT, const u16* __restrict__ kexp,
    const float* __restrict__ kinvp, const u16* __restrict__ w1, const u16* __restrict__ b1, const u16* __restrict__ dskip,
    u16* __restrict__ hbuf, u16* __restrict__ sT, const u16* __restrict__ w2) {
  extern __shared__ __attribute__((aligned(16))) char smem[];
  const int tid = TIDX, lane = tid & 63, w = tid >> 6;
  if (BIDX < G1_BLOCKS) {
    const int bid = BIDX;
    const int xcd = bid & 7, rest = bid >> 3;
    const int mt_i = xcd * 2 + (rest & 1);
    const int nt_i = rest >> 1;
    const int m0 = mt_i * G1_BM, n0 = nt_i * G1_BN;
    const int fr = lane & 15, fq = lane >> 4;
    STAMPK(1, 0);
    const int wu = __builtin_amdgcn_readfirstlane(w);
    const char* gbase = (wu < 2) ? reinterpret_cast<const char*>(xn) : reinterpret_cast<const char*>(w1);
    const unsigned kstepb = (wu < 2) ? (unsigned)(G1_BK * 2) : (unsigned)(G1_BK * FF * 2);
    unsigned off[8];
#pragma unroll
    for (int i = 0; i < 8; ++i) {
      int p = (8 * w + i) * 64 + lane;
      if (wu < 2) {
        int row = p >> 3, ph = p & 7;
        off[i] = (unsigned)(((m0 + row) * DM + (ph ^ (row & 7)) * 8) * 2);
      } else {
        int c = p - 1024; if (c >= 896) c -= 128;
        int kr = c / 14, ch = c - kr * 14;
        off[i] = (unsigned)((kr * FF + n0 + ch * 8) * 2);
      }
    }
    auto issue = [&](int kt) {
      char* base = smem + (kt & 1) * G1_STAGE + (8 * w) * 1024;
      const char* g = gbase + (size_t)kt * kstepb;
#pragma unroll
      for (int i = 0; i < 8; ++i)
        __builtin_amdgcn_global_load_lds((const unsigned*)(g + off[i]),
                                         (unsigned*)(base + i * 1024 + lane * 16), 16, 0, 0);
    };
    u32x2 bbp[7];
#pragma unroll
    for (int j = 0; j < 7; ++j) bbp[j] = *reinterpret_cast<const u32x2*>(b1 + n0 + j * 16 + fq * 4);
    f32x4 acc[2][7];
#pragma unroll
    for (int i = 0; i < 2; ++i)
#pragma unroll
      for (int j = 0; j < 7; ++j) acc[i][j] = (f32x4){0.f, 0.f, 0.f, 0.f};
    constexpr int NKT = DM / G1_BK;
    issue(0);
    issue(1);
    const int q4 = (lane & 15) >> 2, p4 = lane & 3;
    bf16x4 tl[2][2][7], th[2][2][7];
    bf16x8 af[2][2][2];
    auto rdfrag = [&](auto ST) {
      constexpr int st = decltype(ST)::value;
      const char* A = smem + st * G1_STAGE;
      const unsigned baddr = lds_addr(smem) + st * G1_STAGE + G1_ABYTES + (fq * 4 + q4) * G1_BRS + p4 * 8;
      auto rd = [&](auto J) {
        constexpr int j = decltype(J)::value;
        tl[st][0][j] = tr_read<j * 32>(baddr);
        th[st][0][j] = tr_read<j * 32 + 16 * G1_BRS>(baddr);
        tl[st][1][j] = tr_read<j * 32 + 32 * G1_BRS>(baddr);
        th[st][1][j] = tr_read<j * 32 + 48 * G1_BRS>(baddr);
      };
      Unroll<0, 7>::run(rd);
#pragma unroll
      for (int ks = 0; ks < 2; ++ks)
#pragma unroll
        for (int i = 0; i < 2; ++i) {
          int row = w * 32 + i * 16 + fr;
          int c0 = ks * 4 + (fq >> 1), c1 = c0 + 2;
          bf16x4 x0 = *reinterpret_cast<const bf16x4*>(A + row * 128 + ((c0 ^ (row & 7)) << 4) + (fq & 1) * 8);
          bf16x4 x1 = *reinterpret_cast<const bf16x4*>(A + row * 128 + ((c1 ^ (row & 7)) << 4) + (fq & 1) * 8);
          af[st][ks][i] = __builtin_shufflevector(x0, x1, 0, 1, 2, 3, 4, 5, 6, 7);
        }
    };
    auto waitfrag = [&](auto ST) {
      constexpr int st = decltype(ST)::value;
      wait14(tl[st][0], th[st][0]);
      wait14(tl[st][1], th[st][1]);
    };
    auto mma = [&](auto ST) {
      constexpr int st = decltype(ST)::value;
#pragma unroll
      for (int ks = 0; ks < 2; ++ks)
#pragma unroll
        for (int j = 0; j < 7; ++j) {
          bf16x8 bfr = __builtin_shufflevector(tl[st][ks][j], th[st][ks][j], 0, 1, 2, 3, 4, 5, 6, 7);
#pragma unroll
          for (int i = 0; i < 2; ++i)
            acc[i][j] = __builtin_amdgcn_mfma_f32_16x16x32_bf16(bfr, af[st][ks][i], acc[i][j], 0, 0, 0);
        }
    };
    auto step = [&](auto ST, int kt) {
      constexpr int st = decltype(ST)::value;
      wait_vm<0>();
      __builtin_amdgcn_s_barrier();
      if (kt + 2 < NKT) issue(kt + 2);
      rdfrag(std::integral_constant<int, st ^ 1>{});
      mma(ST);
      waitfrag(std::integral_constant<int, st ^ 1>{});
    };
    wait_vm<8>();
    __builtin_amdgcn_s_barrier();
    rdfrag(std::integral_constant<int, 0>{});
    waitfrag(std::integral_constant<int, 0>{});
#pragma unroll 1
    for (int kt = 0; kt < 6; kt += 2) {
      step(std::integral_constant<int, 0>{}, kt);
      step(std::integral_constant<int, 1>{}, kt + 1);
    }
    mma(std::integral_constant<int, 0>{});
    STAMPK(1, 4);
    unsigned warm0, warm1 = 0;
    {
      const int o = (bid >> 3) * 50176 + tid * 128;
      warm0 = *reinterpret_cast<const unsigned*>(reinterpret_cast<const char*>(w2) + o);
      if (tid < 136) warm1 = *reinterpret_cast<const unsigned*>(reinterpret_cast<const char*>(w2) + o + 32768);
    }
    unsigned warm2 = 0;
    {
      unsigned long long ca, ce;
      CODE_ADDR(ca, "_Z8k2_gemm2PKtS0_S0_S0_S0_Pt");
      CODE_ADDR(ce, "_Z10k3_end_tagPj");
      if (ce > ca && (unsigned long long)(tid * 64 + 4) <= ce - ca) warm2 = *reinterpret_cast<const unsigned*>(ca + tid * 64);
    }
    __builtin_amdgcn_sched_barrier(0);
    u32x2 pk[2][7];
#pragma unroll
    for (int i = 0; i < 2; ++i) {
#pragma unroll
      for (int j = 0; j < 7; ++j) {
#pragma unroll
        for (int r = 0; r < 2; ++r) {
          unsigned p0 = pk2bf(acc[i][j][2 * r], acc[i][j][2 * r + 1]);
          unsigned p1 = pk2bf(lo_f(p0) + lo_f(bbp[j][r]), hi_f(p0) + hi_f(bbp[j][r]));
          float t0 = __builtin_fmaxf(lo_f(p1), 0.f), t1 = __builtin_fmaxf(hi_f(p1), 0.f);
          pk[i][j][r] = pk2bf(t0 * t0, t1 * t1);
        }
      }
    }
#pragma unroll
    for (int i = 0; i < 2; ++i) {
      u16* hrow = hbuf + (size_t)(m0 + w * 32 + i * 16 + fr) * HS + n0;
#pragma unroll
      for (int P = 0; P < 3; ++P)
        *reinterpret_cast<u32x4*>(hrow + P * 32 + (fq & 1) * 16 + (fq >> 1) * 8) = pair_cols(pk[i][2 * P], pk[i][2 * P + 1]);
      *reinterpret_cast<u32x2*>(hrow + 96 + fq * 4) = pk[i][6];
    }
    asm volatile("" ::"v"(warm0), "v"(warm1), "v"(warm2));
    STAMPK(1, 5);
    return;
  }
  const int cb = BIDX - G1_BLOCKS;
  const int nch = (cb + 256 < DM) ? 2 : 1;
  char* XS = smem;
  char* KM = smem + CV_XBYTES;
  STAMPK(1, 0);
  u32x4 kv, cur[2], prv[2], xv;
  float dsk, kinv;
  auto prefetch = [&](int d) {
    const u16* xrow = xnT + (size_t)d * SL;
    kv = *reinterpret_cast<const u32x4*>(kexp + (size_t)d * SL + tid * 8);
#pragma unroll
    for (int rep = 0; rep < 2; ++rep) {
      int gg = tid + rep * 256 - 3;
      cur[rep] = (u32x4){0u, 0u, 0u, 0u}; prv[rep] = (u32x4){0u, 0u, 0u, 0u};
      if (rep == 0 || tid < 4) {
        if (gg >= 0 && gg < 256) cur[rep] = *reinterpret_cast<const u32x4*>(xrow + gg * 8);
        if (gg >= 1 && gg <= 256) prv[rep] = *reinterpret_cast<const u32x4*>(xrow + (gg - 1) * 8);
      }
    }
    xv = *reinterpret_cast<const u32x4*>(xrow + tid * 8);
    dsk = bf2f(dskip[d]);
    kinv = kinvp[d];
  };
  prefetch(cb);
  for (int i = tid; i < CV_KPAD * CV_KB / 16; i += 256)
    *reinterpret_cast<u32x4*>(KM + i * 16) = (u32x4){0u, 0u, 0u, 0u};
  for (int ch = 0; ch < nch; ++ch) {
    const int d = cb + ch * 256;
#pragma unroll
    for (int rep = 0; rep < 2; ++rep) {
      if (rep == 0 || tid < 4) {
        int gg = tid + rep * 256 - 3;
        int q = 256 - gg;
        unsigned Dw[8];
#pragma unroll
        for (int k = 0; k < 4; ++k) {
          Dw[k] = pk2h(lo_f(prv[rep][k]), hi_f(prv[rep][k]));
          Dw[4 + k] = pk2h(lo_f(cur[rep][k]), hi_f(cur[rep][k]));
        }
#pragma unroll
        for (int c = 0; c < 8; ++c) {
          u32x4 o;
#pragma unroll
          for (int k = 0; k < 4; ++k) {
            const int la = 8 + c - 2 * k;
            unsigned val;
            if (la & 1) {
              unsigned dd = Dw[(la - 1) >> 1];
              val = (dd >> 16) | (dd << 16);
            } else {
              val = (Dw[la >> 1] & 0xffffu) | (Dw[(la >> 1) - 1] & 0xffff0000u);
            }
            o[k] = val;
          }
          *reinterpret_cast<u32x4*>(XS + c * CV_XS + q * 16) = o;
        }
      }
    }
    {
      const int l0 = tid * 8;
      *reinterpret_cast<u32x4*>(KM + (CV_KPAD + (l0 >> 5)) * CV_KB + (l0 & 31) * 2) = kv;
    }
    const u32x4 xv_c = xv;
    const float dsk_c = dsk, kinv_c = kinv;
    bsync();
    if (ch == 0) STAMPK(1, 2);
    if (ch + 1 < nch) prefetch(d + 256);
    f32x16 acc0, acc1;
#pragma unroll
    for (int i = 0; i < 16; ++i) { acc0[i] = 0.f; acc1[i] = 0.f; }
    {
      const int t0 = lane & 31, hh = lane >> 5;
      const int c = t0 & 7;
      const char* abase = XS + c * CV_XS + 2 * (2048 - 8 * (t0 >> 3) + 8 * hh);
      const char* bbase = KM + (CV_KPAD + t0) * CV_KB + 16 * hh;
      f16x8 fa0[2][2], fa1[2][2], fb0[2][2], fb1[2][2];
      auto ldf = [&](int set, int it) {
        const int dl = w + 4 * it;
#pragma unroll
        for (int ks = 0; ks < 2; ++ks) {
          fa0[set][ks] = *reinterpret_cast<const f16x8*>(abase - 64 * dl + 32 * ks);
          fa1[set][ks] = *reinterpret_cast<const f16x8*>(abase - 64 * (dl + 32) + 32 * ks);
          const char* b0p = bbase - dl * CV_KB + 32 * ks;
          fb0[set][ks] = *reinterpret_cast<const f16x8*>(b0p);
          fb1[set][ks] = *reinterpret_cast<const f16x8*>(b0p + 32 * CV_KB);
        }
      };
      auto mm = [&](int set) {
#pragma unroll
        for (int ks = 0; ks < 2; ++ks) {
          acc0 = __builtin_amdgcn_mfma_f32_32x32x16_f16(fa0[set][ks], fb0[set][ks], acc0, 0, 0, 0);
          acc1 = __builtin_amdgcn_mfma_f32_32x32x16_f16(fa0[set][ks], fb1[set][ks], acc1, 0, 0, 0);
          acc1 = __builtin_amdgcn_mfma_f32_32x32x16_f16(fa1[set][ks], fb0[set][ks], acc1, 0, 0, 0);
        }
      };
      ldf(0, 0);
#pragma unroll 1
      for (int it = 0; it < 6; it += 2) {
        ldf(1, it + 1);
        __builtin_amdgcn_sched_barrier(0);
        mm(0);
        __builtin_amdgcn_sched_barrier(0);
        ldf(0, it + 2);
        __builtin_amdgcn_sched_barrier(0);
        mm(1);
        __builtin_amdgcn_sched_barrier(0);
      }
      ldf(1, 7);
      __builtin_amdgcn_sched_barrier(0);
      mm(0);
      mm(1);
    }
    bsync();
    if (ch == 0) STAMPK(1, 3);
    float* red = reinterpret_cast<float*>(smem);
    {
      const int col = lane & 31, hh = lane >> 5;
#pragma unroll
      for (int g = 0; g < 4; ++g) {
        f32x4 v0 = (f32x4){acc0[4 * g], acc0[4 * g + 1], acc0[4 * g + 2], acc0[4 * g + 3]};
        f32x4 v1 = (f32x4){acc1[4 * g], acc1[4 * g + 1], acc1[4 * g + 2], acc1[4 * g + 3]};
        *reinterpret_cast<f32x4*>(red + w * 2304 + 36 * col + 8 * g + 4 * hh) = v0;
        *reinterpret_cast<f32x4*>(red + w * 2304 + 36 * (32 + col) + 8 * g + 4 * hh) = v1;
      }
    }
    bsync();
    {
      const int t = tid * 8;
      f32x4 y0 = (f32x4){0.f, 0.f, 0.f, 0.f}, y1 = (f32x4){0.f, 0.f, 0.f, 0.f};
#pragma unroll
      for (int ww = 0; ww < 4; ++ww) {
        y0 += *reinterpret_cast<const f32x4*>(red + ww * 2304 + 36 * (tid >> 2) + 8 * (tid & 3));
        y1 += *reinterpret_cast<const f32x4*>(red + ww * 2304 + 36 * (tid >> 2) + 8 * (tid & 3) + 4);
      }
      u32x4 o;
#pragma unroll
      for (int j = 0; j < 4; ++j) {
        float ya = (j < 2 ? y0[2 * j] : y1[2 * j - 4]) * kinv_c;
        float yb = (j < 2 ? y0[2 * j + 1] : y1[2 * j - 3]) * kinv_c;
        o[j] = pk2bf(rbf(ya) + rbf(lo_f(xv_c[j]) * dsk_c), rbf(yb) + rbf(hi_f(xv_c[j]) * dsk_c));
      }
      *reinterpret_cast<u32x4*>(sT + (size_t)d * SL + t) = o;
    }
    if (ch == 0) STAMPK(1, 4);
    if (ch + 1 < nch) bsync();
  }

  STAMPK(1, 5);
}

constexpr int G2_BM = 64, G2_BN = 64, G2_BK = 128;
constexpr int G2_BLOCKS = (SL / G2_BM) * (DM / G2_BN);
constexpr int G2_ABYTES = G2_BM * G2_BK * 2;
constexpr int G2_STAGE = 32768;
#define G2NS 4
constexpr int G2_NS = G2NS;
constexpr int K2_LDS = G2_NS * G2_STAGE;

__device__ __forceinline__ void wait8(bf16x4 (&a)[2][2][2]) {
  asm volatile("s_waitcnt lgkmcnt(0)"
               : "+v"(a[0][0][0]), "+v"(a[0][0][1]), "+v"(a[0][1][0]), "+v"(a[0][1][1]),
                 "+v"(a[1][0][0]), "+v"(a[1][0][1]), "+v"(a[1][1][0]), "+v"(a[1][1][1]));
}
__device__ __forceinline__ int g2_bswz(int r) { return ((r >> 1) & 3) << 1; }

__global__ __launch_bounds__(256, 1) NOPK_ATTR void k2_gemm2(
    const u16* __restrict__ hbuf, const u16* __restrict__ w2, const u16* __restrict__ b2,
    const u16* __restrict__ u, const u16* __restrict__ sT, u16* __restrict__ out) {
  extern __shared__ __attribute__((aligned(16))) char smem[];
  const int tid = TIDX, lane = tid & 63, w = tid >> 6;
  const int bid = BIDX;
  const int xcd = bid & 7, rest = bid >> 3;
  const int mt_i = xcd * 4 + (rest & 3);
  const int nt_i = rest >> 2;
  const int m0 = mt_i * G2_BM, n0 = nt_i * G2_BN;
  const int fr = lane & 15, fq = lane >> 4;
  const int wm = w & 1, wn = w >> 1;
  STAMPK(2, 0);
  const int ncol = wn * 32 + (fq & 1) * 16 + (fq >> 1) * 8;
  u32x4 e_u[2];
  u16 e_s[2][8];
#pragma unroll
  for (int i = 0; i < 2; ++i) {
    const int m = m0 + wm * 32 + i * 16 + fr, n = n0 + ncol;
    e_u[i] = *reinterpret_cast<const u32x4*>(u + (size_t)m * DM + n);
#pragma unroll
    for (int r = 0; r < 8; ++r) e_s[i][r] = sT[(size_t)(n + r) * SL + m];
  }
  float bb[2][4];
#pragma unroll
  for (int j = 0; j < 2; ++j) {
    u32x2 t = *reinterpret_cast<const u32x2*>(b2 + n0 + wn * 32 + j * 16 + fq * 4);
    bb[j][0] = lo_f(t[0]); bb[j][1] = hi_f(t[0]); bb[j][2] = lo_f(t[1]); bb[j][3] = hi_f(t[1]);
  }
  const u16* srcA[2];
  const u16* srcB[2];
#pragma unroll
  for (int i = 0; i < 2; ++i) {
    int p = (2 * w + i) * 64 + lane;
    int row = p >> 3, ph = p & 7;
    srcA[i] = hbuf + (size_t)(m0 + row) * HS + (ph ^ (row & 7)) * 8;
    srcB[i] = w2 + (size_t)row * DM + n0 + (ph ^ g2_bswz(row)) * 8;
  }
  auto issue = [&](int kt) {
    char* base = smem + (kt % G2_NS) * G2_STAGE + (2 * w) * 1024 + lane * 16;
#pragma unroll
    for (int hf = 0; hf < 2; ++hf)
#pragma unroll
      for (int i = 0; i < 2; ++i) {
        __builtin_amdgcn_global_load_lds((const unsigned*)(srcA[i] + kt * G2_BK + hf * 64),
                                         (unsigned*)(base + hf * 8192 + i * 1024), 16, 0, 0);
        __builtin_amdgcn_global_load_lds((const unsigned*)(srcB[i] + (size_t)(kt * G2_BK + hf * 64) * DM),
                                         (unsigned*)(base + G2_ABYTES + hf * 8192 + i * 1024), 16, 0, 0);
      }
  };
  f32x4 acc[2][2];
#pragma unroll
  for (int i = 0; i < 2; ++i)
#pragma unroll
    for (int j = 0; j < 2; ++j) acc[i][j] = (f32x4){0.f, 0.f, 0.f, 0.f};
  constexpr int NKT = FF / G2_BK;
#pragma unroll
  for (int s0 = 0; s0 < G2_NS - 1; ++s0) issue(s0);
  const int q4 = (lane & 15) >> 2, p4 = lane & 3;
  bf16x4 t[2][2][2][2][2];
  bf16x4 xa[2][2][2][2][2];
  auto rdfrag = [&](auto KT) {
    constexpr int kt = decltype(KT)::value;
    constexpr int st = kt & 1;
    const char* A = smem + (kt % G2_NS) * G2_STAGE;
    const unsigned bbase = lds_addr(smem) + (kt % G2_NS) * G2_STAGE + G2_ABYTES;
#pragma unroll
    for (int j = 0; j < 2; ++j)
#pragma unroll
      for (int hf = 0; hf < 2; ++hf) {
        const int r0 = fq * 4 + q4 + 16 * hf;
        const int ch = (wn * 2 + j) * 2 + (p4 >> 1);
        const unsigned a0 = bbase + r0 * 128 + ((ch ^ g2_bswz(r0)) << 4) + (p4 & 1) * 8;
        t[st][0][0][j][hf] = tr_read<0>(a0);
        t[st][0][1][j][hf] = tr_read<32 * 128>(a0);
        t[st][1][0][j][hf] = tr_read<64 * 128>(a0);
        t[st][1][1][j][hf] = tr_read<96 * 128>(a0);
      }
#pragma unroll
    for (int ks = 0; ks < 2; ++ks)
#pragma unroll
      for (int i = 0; i < 2; ++i)
#pragma unroll
        for (int hf = 0; hf < 2; ++hf) {
          int row = wm * 32 + i * 16 + fr;
          int c0 = ks * 4 + (fq >> 1) + 2 * hf;
          const unsigned a0 = lds_addr(A) + row * 128 + ((c0 ^ (row & 7)) << 4) + (fq & 1) * 8;
          xa[st][0][ks][i][hf] = ld_b64<0>(a0);
          xa[st][1][ks][i][hf] = ld_b64<8192>(a0);
        }
  };
  auto waitfrag = [&](auto ST) {
    constexpr int st = decltype(ST)::value;
    wait8(t[st][0]);
    wait8(t[st][1]);
    wait8(xa[st][0]);
    wait8(xa[st][1]);
  };
  wait_vm<8 * (G2_NS - 2)>();
  __builtin_amdgcn_s_barrier();
  STAMPK(2, 1);
  rdfrag(std::integral_constant<int, 0>{});
  waitfrag(std::integral_constant<int, 0>{});
  auto step = [&](auto KT) {
    constexpr int kt = decltype(KT)::value;
    constexpr int st = kt & 1;
    if constexpr (kt + 1 < NKT) {
      constexpr int last = (kt + G2_NS - 2) < (NKT - 1) ? (kt + G2_NS - 2) : (NKT - 1);
      wait_vm<8 * (last - (kt + 1))>();
      __builtin_amdgcn_s_barrier();
      if constexpr (kt == 3) STAMPK(2, 2);
      if constexpr (kt == 7) STAMPK(2, 3);
      if constexpr (kt == 11) STAMPK(2, 4);
      if constexpr (kt + G2_NS - 1 < NKT) issue(kt + G2_NS - 1);
      rdfrag(std::integral_constant<int, kt + 1>{});
    }
#pragma unroll
    for (int hv = 0; hv < 2; ++hv)
#pragma unroll
      for (int ks = 0; ks < 2; ++ks)
#pragma unroll
        for (int j = 0; j < 2; ++j) {
          bf16x8 bfr = __builtin_shufflevector(t[st][hv][ks][j][0], t[st][hv][ks][j][1], 0, 1, 2, 3, 4, 5, 6, 7);
#pragma unroll
          for (int i = 0; i < 2; ++i)
            acc[i][j] = __builtin_amdgcn_mfma_f32_16x16x32_bf16(
                bfr, __builtin_shufflevector(xa[st][hv][ks][i][0], xa[st][hv][ks][i][1], 0, 1, 2, 3, 4, 5, 6, 7),
                acc[i][j], 0, 0, 0);
        }
    if constexpr (kt + 1 < NKT) waitfrag(std::integral_constant<int, st ^ 1>{});
  };
  Unroll<0, NKT>::run(step);
  STAMPK(2, 5);
#pragma unroll
  for (int i = 0; i < 2; ++i) {
    const int m = m0 + wm * 32 + i * 16 + fr, n = n0 + ncol;
    u32x2 gp[2];
#pragma unroll
    for (int j = 0; j < 2; ++j) {
#pragma unroll
      for (int r = 0; r < 2; ++r) {
        unsigned p0 = pk2bf(acc[i][j][2 * r], acc[i][j][2 * r + 1]);
        unsigned p1 = pk2bf(lo_f(p0) + bb[j][2 * r], hi_f(p0) + bb[j][2 * r + 1]);
        float z0 = lo_f(p1), z1 = hi_f(p1);
        gp[j][r] = pk2bf(silu_f(z0), silu_f(z1));
      }
    }
    const u32x4 gv = pair_cols(gp[0], gp[1]);
    u32x4 o;
#pragma unroll
    for (int e = 0; e < 4; ++e)
      o[e] = pk2bf(lo_f(e_u[i][e]) + rbf(bf2f(e_s[i][2 * e]) * lo_f(gv[e])),
                   hi_f(e_u[i][e]) + rbf(bf2f(e_s[i][2 * e + 1]) * hi_f(gv[e])));
    *reinterpret_cast<u32x4*>(out + (size_t)m * DM + n) = o;
  }
  STAMPK(2, 6);
}

__global__ void k3_end_tag(unsigned* p) { p[0] = 0u; }

#if defined(__HIP_DEVICE_COMPILE__)
#pragma clang attribute pop
#endif
extern "C" void kernel_launch(void* const* d_in, const int* in_sizes, int n_in,
                              void* d_out, int out_size, void* d_ws, size_t ws_size,
                              hipStream_t stream) {
  const u16* u = (const u16*)d_in[0];
  const u16* logA = (const u16*)d_in[1];
  const u16* Aim = (const u16*)d_in[2];
  const u16* Cre = (const u16*)d_in[3];
  const u16* Cim = (const u16*)d_in[4];
  const u16* logdt = (const u16*)d_in[5];
  const u16* dskip = (const u16*)d_in[6];
  const u16* w1 = (const u16*)d_in[8];
  const u16* b1 = (const u16*)d_in[9];
  const u16* w2 = (const u16*)d_in[10];
  const u16* b2 = (const u16*)d_in[11];
  u16* out = (u16*)d_out;
  char* ws = (char*)d_ws;
  u16* xn = (u16*)(ws);
  u16* xnT = (u16*)(ws + (2u << 20));
  u16* sT = (u16*)(ws + (4u << 20));
  u16* kexp = (u16*)(ws + (6u << 20));
  float* kinvp = (float*)(ws + (7u << 20) + (900u << 10));
  u16* hbuf = (u16*)(ws + (8u << 20));

  hipFuncSetAttribute((const void*)k0_ln_kgen, hipFuncAttributeMaxDynamicSharedMemorySize, K0_LDS);
  hipFuncSetAttribute((const void*)k1_gemm1_conv, hipFuncAttributeMaxDynamicSharedMemorySize, K1_LDS);
  hipFuncSetAttribute((const void*)k2_gemm2, hipFuncAttributeMaxDynamicSharedMemorySize, K2_LDS);
  k0_ln_kgen<<<LN_BLOCKS + DM, 256, K0_LDS, stream>>>(u, logA, Aim, Cre, Cim, logdt, xn, xnT, kexp, kinvp, w1);
  k1_gemm1_conv<<<G1_BLOCKS + 256, 256, K1_LDS, stream>>>(xn, xnT, kexp, kinvp, w1, b1, dskip, hbuf, sT, w2);
  k2_gemm2<<<G2_BLOCKS, 256, K2_LDS, stream>>>(hbuf, w2, b2, u, sT, out);
}
```

```cpp
#include <hip/hip_runtime.h>
#include <hip/hip_bf16.h>
#include <stdint.h>
#include <type_traits>

#if defined(__HIP_DEVICE_COMPILE__)
#pragma clang attribute push(__attribute__((target("no-packed-fp32-ops"))), apply_to = function)
#endif
typedef unsigned short u16;
typedef __attribute__((ext_vector_type(8))) short bf16x8;
typedef __attribute__((ext_vector_type(4))) short bf16x4;
typedef __attribute__((ext_vector_type(8))) _Float16 f16x8;
typedef __attribute__((ext_vector_type(2))) _Float16 f16x2;
typedef __attribute__((ext_vector_type(2))) __bf16 bf16x2;
typedef __attribute__((ext_vector_type(2))) float f32x2;
typedef __attribute__((ext_vector_type(4))) float f32x4;
typedef __attribute__((ext_vector_type(16))) float f32x16;
typedef __attribute__((ext_vector_type(4))) unsigned int u32x4;
typedef __attribute__((ext_vector_type(2))) unsigned int u32x2;
#define LDS_AS __attribute__((address_space(3)))

#define TIDX ((int)__builtin_amdgcn_workitem_id_x())
#define BIDX ((int)__builtin_amdgcn_workgroup_id_x())
__device__ __forceinline__ float u2f(unsigned v) { return __builtin_bit_cast(float, v); }
__device__ __forceinline__ unsigned f2u(float v) { return __builtin_bit_cast(unsigned, v); }
__device__ __forceinline__ void bsync() {
  __builtin_amdgcn_fence(__ATOMIC_RELEASE, "workgroup");
  __builtin_amdgcn_s_barrier();
  __builtin_amdgcn_fence(__ATOMIC_ACQUIRE, "workgroup");
}
__device__ __forceinline__ float shfl_xor_f(float v, int m, int lane) {
  return u2f((unsigned)__builtin_amdgcn_ds_bpermute((lane ^ m) << 2, (int)f2u(v)));
}
__device__ __forceinline__ float exp_acc(float x) {
  const float L2E_HI = 1.44269502162933349609375f, L2E_LO = 1.92596299112661746e-8f;
  float t = x * L2E_HI;
  float e = __builtin_fmaf(x, L2E_HI, -t) + x * L2E_LO;
  float r = __builtin_amdgcn_exp2f(t);
  return __builtin_fmaf(r, e * 0.693147180559945309f, r);
}

__device__ __forceinline__ float silu_f(float z) {
  float dn = 1.0f + exp_acc(-z);
  float r = __builtin_amdgcn_rcpf(dn);
  float q = z * r;
  float e = __builtin_fmaf(-q, dn, z);
  float res = __builtin_fmaf(e, r, q);
  return (dn < 3.0e38f) ? res : -0.0f * 0.f + (z * 0.f);
}

constexpr int SL = 2048;
constexpr int DM = 448;
constexpr int NMODE = 256;
constexpr int FF = 1792;
#define HPAD 64
constexpr int HS = FF + HPAD;
constexpr int KTAB = 48 * 16;

__device__ __forceinline__ float bf2f(u16 v) { return u2f(((unsigned)v) << 16); }
__device__ __forceinline__ unsigned pk2bf(float a, float b) {
  f32x2 v = {a, b};
  bf16x2 r = __builtin_convertvector(v, bf16x2);
  return *reinterpret_cast<unsigned*>(&r);
}
__device__ __forceinline__ u16 f2bf(float f) { return (u16)(pk2bf(f, 0.f) & 0xffffu); }
__device__ __forceinline__ float rbf(float f) { return u2f(pk2bf(f, 0.f) << 16); }
__device__ __forceinline__ float lo_f(unsigned v) { return u2f(v << 16); }
__device__ __forceinline__ float hi_f(unsigned v) { return u2f(v & 0xffff0000u); }
__device__ __forceinline__ unsigned pk2h(float a, float b) {
  f32x2 v = {a, b};
  f16x2 r = __builtin_convertvector(v, f16x2);
  return *reinterpret_cast<unsigned*>(&r);
}

template <int I, int N> struct Unroll {
  template <class F> static __device__ __forceinline__ void run(F& f) {
    f(std::integral_constant<int, I>{});
    Unroll<I + 1, N>::run(f);
  }
};
template <int N> struct Unroll<N, N> {
  template <class F> static __device__ __forceinline__ void run(F&) {}
};
template <int N> __device__ __forceinline__ void wait_vm() {
  asm volatile("s_waitcnt vmcnt(%0)" ::"n"(N) : "memory");
}
__device__ __forceinline__ void wait_lgkm0() { asm volatile("s_waitcnt lgkmcnt(0)" ::: "memory"); }

template <int OFF> __device__ __forceinline__ bf16x4 tr_read(unsigned addr) {
  bf16x4 r;
  asm volatile("ds_read_b64_tr_b16 %0, %1 offset:%2" : "=v"(r) : "v"(addr), "n"(OFF));
  return r;
}
__device__ __forceinline__ void wait14(bf16x4 (&a)[7], bf16x4 (&b)[7]) {
  asm volatile("s_waitcnt lgkmcnt(0)"
               : "+v"(a[0]), "+v"(a[1]), "+v"(a[2]), "+v"(a[3]), "+v"(a[4]), "+v"(a[5]), "+v"(a[6]),
                 "+v"(b[0]), "+v"(b[1]), "+v"(b[2]), "+v"(b[3]), "+v"(b[4]), "+v"(b[5]), "+v"(b[6]));
}
template <int OFF> __device__ __forceinline__ bf16x4 ld_b64(unsigned addr) {
  bf16x4 r;
  asm volatile("ds_read_b64 %0, %1 offset:%2" : "=v"(r) : "v"(addr), "n"(OFF));
  return r;
}
__device__ __forceinline__ unsigned lds_addr(const void* p) { return (unsigned)(size_t)p; }

__device__ __forceinline__ u32x4 pair_cols(u32x2 a, u32x2 b) {
  auto r0 = __builtin_amdgcn_permlane16_swap(a[0], b[0], false, false);
  auto r1 = __builtin_amdgcn_permlane16_swap(a[1], b[1], false, false);
  return (u32x4){r0[0], r1[0], r0[1], r1[1]};
}
#define CODE_ADDR(var, sym)                                                                   \
  asm volatile("s_getpc_b64 s[20:21]\n\t"                                                     \
               "s_add_u32 s20, s20, " sym "@rel32@lo+4\n\t"                                   \
               "s_addc_u32 s21, s21, " sym "@rel32@hi+12\n\t"                                 \
               "s_mov_b64 %0, s[20:21]"                                                       \
               : "=s"(var) : : "s20", "s21", "scc")
struct cf { float r, i; };
__device__ __forceinline__ cf cmul(cf a, cf b) {
  cf o; o.r = a.r * b.r - a.i * b.i; o.i = a.r * b.i + a.i * b.r; return o;
}
__device__ __forceinline__ cf cexp_p(float ar, float ai, float p) {
  float m = __builtin_amdgcn_exp2f((ar * p) * 1.4426950408889634f);
  float rev = (ai * p) * 0.15915494309189535f;
  rev = __builtin_amdgcn_fractf(rev);
  cf o; o.r = m * __builtin_amdgcn_cosf(rev); o.i = m * __builtin_amdgcn_sinf(rev); return o;
}
template <int CTRL> __device__ __forceinline__ float dpp_add(float s) {
  int t = __builtin_amdgcn_update_dpp(0, (int)f2u(s), CTRL, 0xf, 0xf, true);
  return s + u2f((unsigned)t);
}
__device__ __forceinline__ float red16(float s) {
  s = dpp_add<0xB1>(s);
  s = dpp_add<0x4E>(s);
  s = dpp_add<0x141>(s);
  s = dpp_add<0x140>(s);
  return s;
}

constexpr int LN_ROWS = 16;
constexpr int LN_BLOCKS = SL / LN_ROWS;
constexpr int KG_RS = 136;
constexpr int K0_LDS = 4 * 2 * 16 * KG_RS * 2 + 64;

__device__ __forceinline__ int ktab_index(int l) {
  int pos, r, b;
  if (l < 256) { pos = l; r = ((pos >> 7) << 3) + (pos & 7); b = (pos >> 3) & 15; return r * 16 + b; }
  pos = (int)rbf((float)l);
  if (pos < 512) { int q = pos - 256; r = 16 + ((q >> 7) << 2) + ((q >> 1) & 3); b = (q >> 3) & 15; }
  else if (pos < 1024) { int q = pos - 512; r = 24 + ((q >> 7) << 1) + ((q >> 2) & 1); b = (q >> 3) & 15; }
  else if (pos < 2048) { int q = pos - 1024; r = 32 + (q >> 7); b = (q >> 3) & 15; }
  else { r = 40; b = 0; }
  return r * 16 + b;
}

#define NOPK_ATTR
__global__ __launch_bounds__(256) NOPK_ATTR void k0_ln_kgen(
    const u16* __restrict__ u, const u16* __restrict__ logA, const u16* __restrict__ Aim,
    const u16* __restrict__ Cre, const u16* __restrict__ Cim, const u16* __restrict__ logdt,
    u16* __restrict__ xn, u16* __restrict__ xnT, u16* __restrict__ kexp, float* __restrict__ kinvp,
    const u16* __restrict__ w1) {
  extern __shared__ __attribute__((aligned(16))) char smem[];
  const int tid = TIDX, lane = tid & 63, w = tid >> 6;
  if (BIDX < LN_BLOCKS) {
    u16* tile = reinterpret_cast<u16*>(smem);
    constexpr int TS = DM + 8;
    const int rb = tid >> 4, l16 = tid & 15;
    const int rblk = ((BIDX & 7) << 4) + (BIDX >> 3);
    const int row = rblk * LN_ROWS + rb;
    u32x2 v[7];
#pragma unroll
    for (int i = 0; i < 7; ++i)
      v[i] = *reinterpret_cast<const u32x2*>(u + (size_t)row * DM + i * 64 + l16 * 4);
    float x[28];
#pragma unroll
    for (int i = 0; i < 7; ++i) {
      x[4 * i] = lo_f(v[i][0]); x[4 * i + 1] = hi_f(v[i][0]);
      x[4 * i + 2] = lo_f(v[i][1]); x[4 * i + 3] = hi_f(v[i][1]);
    }
    float s = 0.f;
#pragma unroll
    for (int j = 0; j < 28; ++j) s += x[j];
    s = red16(s);
    const float mean = s * (1.0f / DM);
    float q = 0.f;
#pragma unroll
    for (int j = 0; j < 28; ++j) { float dd = x[j] - mean; q += dd * dd; }
    q = red16(q);
    const float var_b = rbf(q * (1.0f / DM));
    const float mean_b = rbf(mean);
    const float den = rbf(__builtin_sqrtf(rbf(var_b + 1e-5f)));
#pragma unroll
    for (int i = 0; i < 7; ++i) {
      u32x2 o;
      o[0] = pk2bf(rbf(x[4 * i] - mean_b) / den, rbf(x[4 * i + 1] - mean_b) / den);
      o[1] = pk2bf(rbf(x[4 * i + 2] - mean_b) / den, rbf(x[4 * i + 3] - mean_b) / den);
      *reinterpret_cast<u32x2*>(xn + (size_t)row * DM + i * 64 + l16 * 4) = o;
      *reinterpret_cast<u32x2*>(tile + rb * TS + i * 64 + l16 * 4) = o;
    }
    bsync();
    for (int d = tid; d < DM; d += 256) {
      unsigned pk[8];
#pragma unroll
      for (int r = 0; r < 8; ++r)
        pk[r] = (unsigned)tile[(2 * r) * TS + d] | ((unsigned)tile[(2 * r + 1) * TS + d] << 16);
      u32x4* dst = reinterpret_cast<u32x4*>(xnT + (size_t)d * SL + rblk * LN_ROWS);
      dst[0] = (u32x4){pk[0], pk[1], pk[2], pk[3]};
      dst[1] = (u32x4){pk[4], pk[5], pk[6], pk[7]};
    }
    {
      unsigned wv[4];
      const int o = (BIDX >> 3) * 100352 + tid * 128;
#pragma unroll
      for (int q = 0; q < 4; ++q) {
        wv[q] = 0;
        if (q < 3 || tid < 16) wv[q] = *reinterpret_cast<const unsigned*>(reinterpret_cast<const char*>(w1) + o + q * 32768);
      }
      asm volatile("" ::"v"(wv[0]), "v"(wv[1]), "v"(wv[2]), "v"(wv[3]));
      unsigned long long ca, ce;
      CODE_ADDR(ca, "_Z13k1_gemm1_convPKtS0_S0_PKfS0_S0_S0_PtS3_S0_");
      CODE_ADDR(ce, "_Z8k2_gemm2PKtS0_S0_S0_S0_Pt");
      unsigned cv = 0;
      if (ce > ca && (unsigned long long)(tid * 64 + 4) <= ce - ca) cv = *reinterpret_cast<const unsigned*>(ca + tid * 64);
      asm volatile("" ::"v"(cv));
    }
    return;
  }
  const int d = BIDX - LN_BLOCKS;
  u16* wbase = reinterpret_cast<u16*>(smem) + w * (2 * 16 * KG_RS);
  u16* Vb = wbase;
  u16* Wb = wbase + 16 * KG_RS;
  const int idx = d * NMODE + tid;
  const u16 r_ldt = logdt[d], r_la = logA[idx], r_ai = Aim[idx], r_cr = Cre[idx], r_ci = Cim[idx];
  const float dt = rbf(exp_acc(bf2f(r_ldt)));
  const float Are = -rbf(exp_acc(bf2f(r_la)));
  const float ar = Are * dt;
  const float ai = bf2f(r_ai) * dt;
  cf C2; C2.r = 2.0f * (bf2f(r_cr) * dt); C2.i = 2.0f * (bf2f(r_ci) * dt);
  float* RM = reinterpret_cast<float*>(smem + 4 * 2 * 16 * KG_RS * 2);
  float wsc, wsc_inv;
  {
    float m = __builtin_fmaxf(__builtin_fabsf(C2.r), __builtin_fabsf(C2.i));
    float sm = __builtin_fabsf(C2.r) + __builtin_fabsf(C2.i);
#pragma unroll
    for (int o = 32; o > 0; o >>= 1) { m = __builtin_fmaxf(m, shfl_xor_f(m, o, lane)); sm += shfl_xor_f(sm, o, lane); }
    if (lane == 0) RM[w] = sm;
    int e = (int)((f2u(m) >> 23) & 0xff) - 127;
    if (m == 0.f || e > 100 || e < -100) e = 13;
    wsc = u2f((unsigned)(127 + 13 - e) << 23);
    wsc_inv = u2f((unsigned)(127 - 13 + e) << 23);
  }
  C2.r *= wsc; C2.i *= wsc;
  auto st = [&](u16* buf, int row, float re, float im) {
    *reinterpret_cast<unsigned*>(buf + row * KG_RS + 2 * lane) = pk2h(re, im);
  };
  {
    cf V1 = cexp_p(ar, ai, 8.f);
    cf V8 = cexp_p(ar, ai, 64.f);
    cf V[8];
    V[0].r = 1.f; V[0].i = 0.f;
    V[1] = V1;
    V[2] = cmul(V1, V1);
    V[3] = cmul(V[2], V1);
    V[4] = cmul(V[2], V[2]);
    V[5] = cmul(V[4], V1);
    V[6] = cmul(V[4], V[2]);
    V[7] = cmul(V[4], V[3]);
#pragma unroll
    for (int b = 0; b < 8; ++b) st(Vb, b, V[b].r, V[b].i);
    st(Vb, 8, V8.r, V8.i);
#pragma unroll
    for (int b = 1; b < 8; ++b) { cf t = cmul(V8, V[b]); st(Vb, 8 + b, t.r, t.i); }
  }
  cf U[8];
  {
    cf E1 = cexp_p(ar, ai, 1.f);
    cf E2 = cmul(E1, E1);
    cf E3 = cmul(E2, E1);
    cf E4 = cmul(E2, E2);
    U[0] = C2;
    U[1] = cmul(C2, E1);
    U[2] = cmul(C2, E2);
    U[3] = cmul(C2, E3);
    U[4] = cmul(C2, E4);
    U[5] = cmul(U[4], E1);
    U[6] = cmul(U[4], E2);
    U[7] = cmul(U[4], E3);
  }
  const cf F1 = cexp_p(ar, ai, 128.f);
  const cf F2 = cexp_p(ar, ai, 256.f);
  const cf F4 = cexp_p(ar, ai, 512.f);
  const cf F8 = cexp_p(ar, ai, 1024.f);
  const cf F3 = cmul(F2, F1);

  f32x4 acc[3];
#pragma unroll
  for (int mt = 0; mt < 3; ++mt) acc[mt] = (f32x4){0.f, 0.f, 0.f, 0.f};
  const int fr = lane & 15, fq = lane >> 4;
  asm volatile("" ::: "memory");
  f16x8 bv[4];
#pragma unroll
  for (int s4 = 0; s4 < 4; ++s4)
    bv[s4] = *reinterpret_cast<const f16x8*>(Vb + fr * KG_RS + 32 * s4 + 8 * fq);
  auto tile = [&](int mt, const cf* T) {
    asm volatile("" ::: "memory");
#pragma unroll
    for (int r = 0; r < 16; ++r) st(Wb, r, T[r].r, -T[r].i);
    asm volatile("" ::: "memory");
#pragma unroll
    for (int s4 = 0; s4 < 4; ++s4) {
      f16x8 a = *reinterpret_cast<const f16x8*>(Wb + fr * KG_RS + 32 * s4 + 8 * fq);
      acc[mt] = __builtin_amdgcn_mfma_f32_16x16x32_f16(a, bv[s4], acc[mt], 0, 0, 0);
    }
    asm volatile("" ::: "memory");
  };
  cf T[16];
#pragma unroll
  for (int e = 0; e < 8; ++e) { T[e] = U[e]; T[8 + e] = cmul(F1, U[e]); }
  tile(0, T);
  cf G[4];
  G[0] = F4; G[1] = cmul(F4, F1); G[2] = cmul(F4, F2); G[3] = cmul(F4, F3);
#pragma unroll
  for (int e = 0; e < 4; ++e) {
    T[e] = cmul(F2, U[2 * e]);
    T[4 + e] = cmul(F3, U[2 * e]);
    T[8 + 2 * e] = cmul(G[e], U[0]);
    T[8 + 2 * e + 1] = cmul(G[e], U[4]);
  }
  tile(1, T);
  {
    cf H0 = cmul(F8, U[0]);
    T[0] = H0; T[1] = cmul(H0, F1); T[2] = cmul(H0, F2); T[3] = cmul(H0, F3);
#pragma unroll
    for (int a2 = 0; a2 < 4; ++a2) T[4 + a2] = cmul(H0, G[a2]);
    T[8] = cmul(H0, F8);
#pragma unroll
    for (int r = 9; r < 16; ++r) { T[r].r = 0.f; T[r].i = 0.f; }
  }
  tile(2, T);
#pragma unroll
  for (int mt = 0; mt < 3; ++mt) acc[mt] *= wsc_inv;
  bsync();
  float* red = reinterpret_cast<float*>(smem);
#pragma unroll
  for (int mt = 0; mt < 3; ++mt)
#pragma unroll
    for (int j = 0; j < 4; ++j) red[w * KTAB + (mt * 16 + fq * 4 + j) * 16 + fr] = acc[mt][j];
  bsync();
  float* KT = red + 4 * KTAB;
#pragma unroll
  for (int r = 0; r < 3; ++r) {
    int i = tid + r * 256;
    KT[i] = (red[i] + red[KTAB + i]) + (red[2 * KTAB + i] + red[3 * KTAB + i]);
  }
  bsync();
  {
    float m = (RM[0] + RM[1]) + (RM[2] + RM[3]);
    int e = (int)((f2u(m) >> 23) & 0xff) - 127;
    if (m == 0.f || e > 100 || e < -100) e = 14;
    const float kscale = u2f((unsigned)(127 + 14 - e) << 23);
    if (tid == 0) kinvp[d] = u2f((unsigned)(127 - 14 + e) << 23);
    const int l0 = tid * 8;
    u32x4 hv;
#pragma unroll
    for (int j = 0; j < 4; ++j) {
      float v0 = KT[ktab_index(l0 + 2 * j)], v1 = KT[ktab_index(l0 + 2 * j + 1)];
      hv[j] = pk2h(v0 * kscale, v1 * kscale);
    }
    *reinterpret_cast<u32x4*>(kexp + (size_t)d * SL + l0) = hv;
  }

}

constexpr int G1_BM = 128, G1_BN = 112, G1_BK = 64;
constexpr int G1_BLOCKS = (SL / G1_BM) * (FF / G1_BN);
constexpr int G1_ABYTES = G1_BM * G1_BK * 2;
constexpr int G1_BRS = 224;
constexpr int G1_STAGE = 32768;
constexpr int G1_NS = 2;
constexpr int CV_XS = 4160;
constexpr int CV_XBYTES = 36864;
constexpr int CV_KB = 80;
constexpr int CV_KPAD = 31;
constexpr int CV_KBYTES = (CV_KPAD + 64) * CV_KB;
constexpr int K1_LDS = 81920;

__global__ __launch_bounds__(256, 2) NOPK_ATTR void k1_gemm1_conv(
    const u16* __restrict__ xn, const u16* __restrict__ xnT, const u16* __restrict__ kexp,
    const float* __restrict__ kinvp, const u16* __restrict__ w1, const u16* __restrict__ b1, const u16* __restrict__ dskip,
    u16* __restrict__ hbuf, u16* __restrict__ sT, const u16* __restrict__ w2) {
  extern __shared__ __attribute__((aligned(16))) char smem[];
  const int tid = TIDX, lane = tid & 63, w = tid >> 6;
  if (BIDX < G1_BLOCKS) {
    const int bid = BIDX;
    const int xcd = bid & 7, rest = bid >> 3;
    const int mt_i = xcd * 2 + (rest & 1);
    const int nt_i = rest >> 1;
    const int m0 = mt_i * G1_BM, n0 = nt_i * G1_BN;
    const int fr = lane & 15, fq = lane >> 4;
    const int wu = __builtin_amdgcn_readfirstlane(w);
    const char* gbase = (wu < 2) ? reinterpret_cast<const char*>(xn) : reinterpret_cast<const char*>(w1);
    const unsigned kstepb = (wu < 2) ? (unsigned)(G1_BK * 2) : (unsigned)(G1_BK * FF * 2);
    unsigned off[8];
#pragma unroll
    for (int i = 0; i < 8; ++i) {
      int p = (8 * w + i) * 64 + lane;
      if (wu < 2) {
        int row = p >> 3, ph = p & 7;
        off[i] = (unsigned)(((m0 + row) * DM + (ph ^ (row & 7)) * 8) * 2);
      } else {
        int c = p - 1024; if (c >= 896) c -= 128;
        int kr = c / 14, ch = c - kr * 14;
        off[i] = (unsigned)((kr * FF + n0 + ch * 8) * 2);
      }
    }
    auto issue = [&](int kt) {
      char* base = smem + (kt & 1) * G1_STAGE + (8 * w) * 1024;
      const char* g = gbase + (size_t)kt * kstepb;
#pragma unroll
      for (int i = 0; i < 8; ++i)
        __builtin_amdgcn_global_load_lds((const unsigned*)(g + off[i]),
                                         (unsigned*)(base + i * 1024 + lane * 16), 16, 0, 0);
    };
    u32x2 bbp[7];
#pragma unroll
    for (int j = 0; j < 7; ++j) bbp[j] = *reinterpret_cast<const u32x2*>(b1 + n0 + j * 16 + fq * 4);
    f32x4 acc[2][7];
#pragma unroll
    for (int i = 0; i < 2; ++i)
#pragma unroll
      for (int j = 0; j < 7; ++j) acc[i][j] = (f32x4){0.f, 0.f, 0.f, 0.f};
    constexpr int NKT = DM / G1_BK;
    issue(0);
    issue(1);
    const int q4 = (lane & 15) >> 2, p4 = lane & 3;
    bf16x4 tl[2][2][7], th[2][2][7];
    bf16x8 af[2][2][2];
    auto rdfrag = [&](auto ST) {
      constexpr int st = decltype(ST)::value;
      const char* A = smem + st * G1_STAGE;
      const unsigned baddr = lds_addr(smem) + st * G1_STAGE + G1_ABYTES + (fq * 4 + q4) * G1_BRS + p4 * 8;
      auto rd = [&](auto J) {
        constexpr int j = decltype(J)::value;
        tl[st][0][j] = tr_read<j * 32>(baddr);
        th[st][0][j] = tr_read<j * 32 + 16 * G1_BRS>(baddr);
        tl[st][1][j] = tr_read<j * 32 + 32 * G1_BRS>(baddr);
        th[st][1][j] = tr_read<j * 32 + 48 * G1_BRS>(baddr);
      };
      Unroll<0, 7>::run(rd);
#pragma unroll
      for (int ks = 0; ks < 2; ++ks)
#pragma unroll
        for (int i = 0; i < 2; ++i) {
          int row = w * 32 + i * 16 + fr;
          int c0 = ks * 4 + (fq >> 1), c1 = c0 + 2;
          bf16x4 x0 = *reinterpret_cast<const bf16x4*>(A + row * 128 + ((c0 ^ (row & 7)) << 4) + (fq & 1) * 8);
          bf16x4 x1 = *reinterpret_cast<const bf16x4*>(A + row * 128 + ((c1 ^ (row & 7)) << 4) + (fq & 1) * 8);
          af[st][ks][i] = __builtin_shufflevector(x0, x1, 0, 1, 2, 3, 4, 5, 6, 7);
        }
    };
    auto waitfrag = [&](auto ST) {
      constexpr int st = decltype(ST)::value;
      wait14(tl[st][0], th[st][0]);
      wait14(tl[st][1], th[st][1]);
    };
    auto mma = [&](auto ST) {
      constexpr int st = decltype(ST)::value;
#pragma unroll
      for (int ks = 0; ks < 2; ++ks)
#pragma unroll
        for (int j = 0; j < 7; ++j) {
          bf16x8 bfr = __builtin_shufflevector(tl[st][ks][j], th[st][ks][j], 0, 1, 2, 3, 4, 5, 6, 7);
#pragma unroll
          for (int i = 0; i < 2; ++i)
            acc[i][j] = __builtin_amdgcn_mfma_f32_16x16x32_bf16(bfr, af[st][ks][i], acc[i][j], 0, 0, 0);
        }
    };
    auto step = [&](auto ST, int kt) {
      constexpr int st = decltype(ST)::value;
      wait_vm<0>();
      __builtin_amdgcn_s_barrier();
      if (kt + 2 < NKT) issue(kt + 2);
      rdfrag(std::integral_constant<int, st ^ 1>{});
      mma(ST);
      waitfrag(std::integral_constant<int, st ^ 1>{});
    };
    wait_vm<8>();
    __builtin_amdgcn_s_barrier();
    rdfrag(std::integral_constant<int, 0>{});
    waitfrag(std::integral_constant<int, 0>{});
#pragma unroll 1
    for (int kt = 0; kt < 6; kt += 2) {
      step(std::integral_constant<int, 0>{}, kt);
      step(std::integral_constant<int, 1>{}, kt + 1);
    }
    mma(std::integral_constant<int, 0>{});
    unsigned warm0, warm1 = 0;
    {
      const int o = (bid >> 3) * 50176 + tid * 128;
      warm0 = *reinterpret_cast<const unsigned*>(reinterpret_cast<const char*>(w2) + o);
      if (tid < 136) warm1 = *reinterpret_cast<const unsigned*>(reinterpret_cast<const char*>(w2) + o + 32768);
    }
    unsigned warm2 = 0;
    {
      unsigned long long ca, ce;
      CODE_ADDR(ca, "_Z8k2_gemm2PKtS0_S0_S0_S0_Pt");
      CODE_ADDR(ce, "_Z10k3_end_tagPj");
      if (ce > ca && (unsigned long long)(tid * 64 + 4) <= ce - ca) warm2 = *reinterpret_cast<const unsigned*>(ca + tid * 64);
    }
    __builtin_amdgcn_sched_barrier(0);
    u32x2 pk[2][7];
#pragma unroll
    for (int i = 0; i < 2; ++i) {
#pragma unroll
      for (int j = 0; j < 7; ++j) {
#pragma unroll
        for (int r = 0; r < 2; ++r) {
          unsigned p0 = pk2bf(acc[i][j][2 * r], acc[i][j][2 * r + 1]);
          unsigned p1 = pk2bf(lo_f(p0) + lo_f(bbp[j][r]), hi_f(p0) + hi_f(bbp[j][r]));
          float t0 = __builtin_fmaxf(lo_f(p1), 0.f), t1 = __builtin_fmaxf(hi_f(p1), 0.f);
          pk[i][j][r] = pk2bf(t0 * t0, t1 * t1);
        }
      }
    }
#pragma unroll
    for (int i = 0; i < 2; ++i) {
      u16* hrow = hbuf + (size_t)(m0 + w * 32 + i * 16 + fr) * HS + n0;
#pragma unroll
      for (int P = 0; P < 3; ++P)
        *reinterpret_cast<u32x4*>(hrow + P * 32 + (fq & 1) * 16 + (fq >> 1) * 8) = pair_cols(pk[i][2 * P], pk[i][2 * P + 1]);
      *reinterpret_cast<u32x2*>(hrow + 96 + fq * 4) = pk[i][6];
    }
    asm volatile("" ::"v"(warm0), "v"(warm1), "v"(warm2));
    return;
  }
  const int cb = BIDX - G1_BLOCKS;
  const int nch = (cb + 256 < DM) ? 2 : 1;
  char* XS = smem;
  char* KM = smem + CV_XBYTES;
  u32x4 kv, cur[2], prv[2], xv;
  float dsk, kinv;
  auto prefetch = [&](int d) {
    const u16* xrow = xnT + (size_t)d * SL;
    kv = *reinterpret_cast<const u32x4*>(kexp + (size_t)d * SL + tid * 8);
#pragma unroll
    for (int rep = 0; rep < 2; ++rep) {
      int gg = tid + rep * 256 - 3;
      cur[rep] = (u32x4){0u, 0u, 0u, 0u}; prv[rep] = (u32x4){0u, 0u, 0u, 0u};
      if (rep == 0 || tid < 4) {
        if (gg >= 0 && gg < 256) cur[rep] = *reinterpret_cast<const u32x4*>(xrow + gg * 8);
        if (gg >= 1 && gg <= 256) prv[rep] = *reinterpret_cast<const u32x4*>(xrow + (gg - 1) * 8);
      }
    }
    xv = *reinterpret_cast<const u32x4*>(xrow + tid * 8);
    dsk = bf2f(dskip[d]);
    kinv = kinvp[d];
  };
  prefetch(cb);
  for (int i = tid; i < CV_KPAD * CV_KB / 16; i += 256)
    *reinterpret_cast<u32x4*>(KM + i * 16) = (u32x4){0u, 0u, 0u, 0u};
  for (int ch = 0; ch < nch; ++ch) {
    const int d = cb + ch * 256;
#pragma unroll
    for (int rep = 0; rep < 2; ++rep) {
      if (rep == 0 || tid < 4) {
        int gg = tid + rep * 256 - 3;
        int q = 256 - gg;
        unsigned Dw[8];
#pragma unroll
        for (int k = 0; k < 4; ++k) {
          Dw[k] = pk2h(lo_f(prv[rep][k]), hi_f(prv[rep][k]));
          Dw[4 + k] = pk2h(lo_f(cur[rep][k]), hi_f(cur[rep][k]));
        }
#pragma unroll
        for (int c = 0; c < 8; ++c) {
          u32x4 o;
#pragma unroll
          for (int k = 0; k < 4; ++k) {
            const int la = 8 + c - 2 * k;
            unsigned val;
            if (la & 1) {
              unsigned dd = Dw[(la - 1) >> 1];
              val = (dd >> 16) | (dd << 16);
            } else {
              val = (Dw[la >> 1] & 0xffffu) | (Dw[(la >> 1) - 1] & 0xffff0000u);
            }
            o[k] = val;
          }
          *reinterpret_cast<u32x4*>(XS + c * CV_XS + q * 16) = o;
        }
      }
    }
    {
      const int l0 = tid * 8;
      *reinterpret_cast<u32x4*>(KM + (CV_KPAD + (l0 >> 5)) * CV_KB + (l0 & 31) * 2) = kv;
    }
    const u32x4 xv_c = xv;
    const float dsk_c = dsk, kinv_c = kinv;
    bsync();
    if (ch + 1 < nch) prefetch(d + 256);
    f32x16 acc0, acc1;
#pragma unroll
    for (int i = 0; i < 16; ++i) { acc0[i] = 0.f; acc1[i] = 0.f; }
    {
      const int t0 = lane & 31, hh = lane >> 5;
      const int c = t0 & 7;
      const char* abase = XS + c * CV_XS + 2 * (2048 - 8 * (t0 >> 3) + 8 * hh);
      const char* bbase = KM + (CV_KPAD + t0) * CV_KB + 16 * hh;
      f16x8 fa0[2][2], fa1[2][2], fb0[2][2], fb1[2][2];
      auto ldf = [&](int set, int it) {
        const int dl = w + 4 * it;
#pragma unroll
        for (int ks = 0; ks < 2; ++ks) {
          fa0[set][ks] = *reinterpret_cast<const f16x8*>(abase - 64 * dl + 32 * ks);
          fa1[set][ks] = *reinterpret_cast<const f16x8*>(abase - 64 * (dl + 32) + 32 * ks);
          const char* b0p = bbase - dl * CV_KB + 32 * ks;
          fb0[set][ks] = *reinterpret_cast<const f16x8*>(b0p);
          fb1[set][ks] = *reinterpret_cast<const f16x8*>(b0p + 32 * CV_KB);
        }
      };
      auto mm = [&](int set) {
#pragma unroll
        for (int ks = 0; ks < 2; ++ks) {
          acc0 = __builtin_amdgcn_mfma_f32_32x32x16_f16(fa0[set][ks], fb0[set][ks], acc0, 0, 0, 0);
          acc1 = __builtin_amdgcn_mfma_f32_32x32x16_f16(fa0[set][ks], fb1[set][ks], acc1, 0, 0, 0);
          acc1 = __builtin_amdgcn_mfma_f32_32x32x16_f16(fa1[set][ks], fb0[set][ks], acc1, 0, 0, 0);
        }
      };
      ldf(0, 0);
#pragma unroll 1
      for (int it = 0; it < 6; it += 2) {
        ldf(1, it + 1);
        __builtin_amdgcn_sched_barrier(0);
        mm(0);
        __builtin_amdgcn_sched_barrier(0);
        ldf(0, it + 2);
        __builtin_amdgcn_sched_barrier(0);
        mm(1);
        __builtin_amdgcn_sched_barrier(0);
      }
      ldf(1, 7);
      __builtin_amdgcn_sched_barrier(0);
      mm(0);
      mm(1);
    }
    bsync();
    float* red = reinterpret_cast<float*>(smem);
    {
      const int col = lane & 31, hh = lane >> 5;
#pragma unroll
      for (int g = 0; g < 4; ++g) {
        f32x4 v0 = (f32x4){acc0[4 * g], acc0[4 * g + 1], acc0[4 * g + 2], acc0[4 * g + 3]};
        f32x4 v1 = (f32x4){acc1[4 * g], acc1[4 * g + 1], acc1[4 * g + 2], acc1[4 * g + 3]};
        *reinterpret_cast<f32x4*>(red + w * 2304 + 36 * col + 8 * g + 4 * hh) = v0;
        *reinterpret_cast<f32x4*>(red + w * 2304 + 36 * (32 + col) + 8 * g + 4 * hh) = v1;
      }
    }
    bsync();
    {
      const int t = tid * 8;
      f32x4 y0 = (f32x4){0.f, 0.f, 0.f, 0.f}, y1 = (f32x4){0.f, 0.f, 0.f, 0.f};
#pragma unroll
      for (int ww = 0; ww < 4; ++ww) {
        y0 += *reinterpret_cast<const f32x4*>(red + ww * 2304 + 36 * (tid >> 2) + 8 * (tid & 3));
        y1 += *reinterpret_cast<const f32x4*>(red + ww * 2304 + 36 * (tid >> 2) + 8 * (tid & 3) + 4);
      }
      u32x4 o;
#pragma unroll
      for (int j = 0; j < 4; ++j) {
        float ya = (j < 2 ? y0[2 * j] : y1[2 * j - 4]) * kinv_c;
        float yb = (j < 2 ? y0[2 * j + 1] : y1[2 * j - 3]) * kinv_c;
        o[j] = pk2bf(rbf(ya) + rbf(lo_f(xv_c[j]) * dsk_c), rbf(yb) + rbf(hi_f(xv_c[j]) * dsk_c));
      }
      *reinterpret_cast<u32x4*>(sT + (size_t)d * SL + t) = o;
    }
    if (ch + 1 < nch) bsync();
  }

}

constexpr int G2_BM = 64, G2_BN = 64, G2_BK = 128;
constexpr int G2_BLOCKS = (SL / G2_BM) * (DM / G2_BN);
constexpr int G2_ABYTES = G2_BM * G2_BK * 2;
constexpr int G2_STAGE = 32768;
#define G2NS 4
constexpr int G2_NS = G2NS;
constexpr int K2_LDS = G2_NS * G2_STAGE;

__device__ __forceinline__ void wait8(bf16x4 (&a)[2][2][2]) {
  asm volatile("s_waitcnt lgkmcnt(0)"
               : "+v"(a[0][0][0]), "+v"(a[0][0][1]), "+v"(a[0][1][0]), "+v"(a[0][1][1]),
                 "+v"(a[1][0][0]), "+v"(a[1][0][1]), "+v"(a[1][1][0]), "+v"(a[1][1][1]));
}
__device__ __forceinline__ int g2_bswz(int r) { return ((r >> 1) & 3) << 1; }

__global__ __launch_bounds__(256, 1) NOPK_ATTR void k2_gemm2(
    const u16* __restrict__ hbuf, const u16* __restrict__ w2, const u16* __restrict__ b2,
    const u16* __restrict__ u, const u16* __restrict__ sT, u16* __restrict__ out) {
  extern __shared__ __attribute__((aligned(16))) char smem[];
  const int tid = TIDX, lane = tid & 63, w = tid >> 6;
  const int bid = BIDX;
  const int xcd = bid & 7, rest = bid >> 3;
  const int mt_i = xcd * 4 + (rest & 3);
  const int nt_i = rest >> 2;
  const int m0 = mt_i * G2_BM, n0 = nt_i * G2_BN;
  const int fr = lane & 15, fq = lane >> 4;
  const int wm = w & 1, wn = w >> 1;
  const int ncol = wn * 32 + (fq & 1) * 16 + (fq >> 1) * 8;
  u32x4 e_u[2];
  u16 e_s[2][8];
#pragma unroll
  for (int i = 0; i < 2; ++i) {
    const int m = m0 + wm * 32 + i * 16 + fr, n = n0 + ncol;
    e_u[i] = *reinterpret_cast<const u32x4*>(u + (size_t)m * DM + n);
#pragma unroll
    for (int r = 0; r < 8; ++r) e_s[i][r] = sT[(size_t)(n + r) * SL + m];
  }
  float bb[2][4];
#pragma unroll
  for (int j = 0; j < 2; ++j) {
    u32x2 t = *reinterpret_cast<const u32x2*>(b2 + n0 + wn * 32 + j * 16 + fq * 4);
    bb[j][0] = lo_f(t[0]); bb[j][1] = hi_f(t[0]); bb[j][2] = lo_f(t[1]); bb[j][3] = hi_f(t[1]);
  }
  const u16* srcA[2];
  const u16* srcB[2];
#pragma unroll
  for (int i = 0; i < 2; ++i) {
    int p = (2 * w + i) * 64 + lane;
    int row = p >> 3, ph = p & 7;
    srcA[i] = hbuf + (size_t)(m0 + row) * HS + (ph ^ (row & 7)) * 8;
    srcB[i] = w2 + (size_t)row * DM + n0 + (ph ^ g2_bswz(row)) * 8;
  }
  auto issue = [&](int kt) {
    char* base = smem + (kt % G2_NS) * G2_STAGE + (2 * w) * 1024 + lane * 16;
#pragma unroll
    for (int hf = 0; hf < 2; ++hf)
#pragma unroll
      for (int i = 0; i < 2; ++i) {
        __builtin_amdgcn_global_load_lds((const unsigned*)(srcA[i] + kt * G2_BK + hf * 64),
                                         (unsigned*)(base + hf * 8192 + i * 1024), 16, 0, 0);
        __builtin_amdgcn_global_load_lds((const unsigned*)(srcB[i] + (size_t)(kt * G2_BK + hf * 64) * DM),
                                         (unsigned*)(base + G2_ABYTES + hf * 8192 + i * 1024), 16, 0, 0);
      }
  };
  f32x4 acc[2][2];
#pragma unroll
  for (int i = 0; i < 2; ++i)
#pragma unroll
    for (int j = 0; j < 2; ++j) acc[i][j] = (f32x4){0.f, 0.f, 0.f, 0.f};
  constexpr int NKT = FF / G2_BK;
#pragma unroll
  for (int s0 = 0; s0 < G2_NS - 1; ++s0) issue(s0);
  const int q4 = (lane & 15) >> 2, p4 = lane & 3;
  bf16x4 t[2][2][2][2][2];
  bf16x4 xa[2][2][2][2][2];
  auto rdfrag = [&](auto KT) {
    constexpr int kt = decltype(KT)::value;
    constexpr int st = kt & 1;
    const char* A = smem + (kt % G2_NS) * G2_STAGE;
    const unsigned bbase = lds_addr(smem) + (kt % G2_NS) * G2_STAGE + G2_ABYTES;
#pragma unroll
    for (int j = 0; j < 2; ++j)
#pragma unroll
      for (int hf = 0; hf < 2; ++hf) {
        const int r0 = fq * 4 + q4 + 16 * hf;
        const int ch = (wn * 2 + j) * 2 + (p4 >> 1);
        const unsigned a0 = bbase + r0 * 128 + ((ch ^ g2_bswz(r0)) << 4) + (p4 & 1) * 8;
        t[st][0][0][j][hf] = tr_read<0>(a0);
        t[st][0][1][j][hf] = tr_read<32 * 128>(a0);
        t[st][1][0][j][hf] = tr_read<64 * 128>(a0);
        t[st][1][1][j][hf] = tr_read<96 * 128>(a0);
      }
#pragma unroll
    for (int ks = 0; ks < 2; ++ks)
#pragma unroll
      for (int i = 0; i < 2; ++i)
#pragma unroll
        for (int hf = 0; hf < 2; ++hf) {
          int row = wm * 32 + i * 16 + fr;
          int c0 = ks * 4 + (fq >> 1) + 2 * hf;
          const unsigned a0 = lds_addr(A) + row * 128 + ((c0 ^ (row & 7)) << 4) + (fq & 1) * 8;
          xa[st][0][ks][i][hf] = ld_b64<0>(a0);
          xa[st][1][ks][i][hf] = ld_b64<8192>(a0);
        }
  };
  auto waitfrag = [&](auto ST) {
    constexpr int st = decltype(ST)::value;
    wait8(t[st][0]);
    wait8(t[st][1]);
    wait8(xa[st][0]);
    wait8(xa[st][1]);
  };
  wait_vm<8 * (G2_NS - 2)>();
  __builtin_amdgcn_s_barrier();
  rdfrag(std::integral_constant<int, 0>{});
  waitfrag(std::integral_constant<int, 0>{});
  auto step = [&](auto KT) {
    constexpr int kt = decltype(KT)::value;
    constexpr int st = kt & 1;
    if constexpr (kt + 1 < NKT) {
      constexpr int last = (kt + G2_NS - 2) < (NKT - 1) ? (kt + G2_NS - 2) : (NKT - 1);
      wait_vm<8 * (last - (kt + 1))>();
      __builtin_amdgcn_s_barrier();
      if constexpr (kt + G2_NS - 1 < NKT) issue(kt + G2_NS - 1);
      rdfrag(std::integral_constant<int, kt + 1>{});
    }
#pragma unroll
    for (int hv = 0; hv < 2; ++hv)
#pragma unroll
      for (int ks = 0; ks < 2; ++ks)
#pragma unroll
        for (int j = 0; j < 2; ++j) {
          bf16x8 bfr = __builtin_shufflevector(t[st][hv][ks][j][0], t[st][hv][ks][j][1], 0, 1, 2, 3, 4, 5, 6, 7);
#pragma unroll
          for (int i = 0; i < 2; ++i)
            acc[i][j] = __builtin_amdgcn_mfma_f32_16x16x32_bf16(
                bfr, __builtin_shufflevector(xa[st][hv][ks][i][0], xa[st][hv][ks][i][1], 0, 1, 2, 3, 4, 5, 6, 7),
                acc[i][j], 0, 0, 0);
        }
    if constexpr (kt + 1 < NKT) waitfrag(std::integral_constant<int, st ^ 1>{});
  };
  Unroll<0, NKT>::run(step);
#pragma unroll
  for (int i = 0; i < 2; ++i) {
    const int m = m0 + wm * 32 + i * 16 + fr, n = n0 + ncol;
    u32x2 gp[2];
#pragma unroll
    for (int j = 0; j < 2; ++j) {
#pragma unroll
      for (int r = 0; r < 2; ++r) {
        unsigned p0 = pk2bf(acc[i][j][2 * r], acc[i][j][2 * r + 1]);
        unsigned p1 = pk2bf(lo_f(p0) + bb[j][2 * r], hi_f(p0) + bb[j][2 * r + 1]);
        float z0 = lo_f(p1), z1 = hi_f(p1);
        gp[j][r] = pk2bf(silu_f(z0), silu_f(z1));
      }
    }
    const u32x4 gv = pair_cols(gp[0], gp[1]);
    u32x4 o;
#pragma unroll
    for (int e = 0; e < 4; ++e)
      o[e] = pk2bf(lo_f(e_u[i][e]) + rbf(bf2f(e_s[i][2 * e]) * lo_f(gv[e])),
                   hi_f(e_u[i][e]) + rbf(bf2f(e_s[i][2 * e + 1]) * hi_f(gv[e])));
    *reinterpret_cast<u32x4*>(out + (size_t)m * DM + n) = o;
  }
}

__global__ void k3_end_tag(unsigned* p) { p[0] = 0u; }

#if defined(__HIP_DEVICE_COMPILE__)
#pragma clang attribute pop
#endif
extern "C" void kernel_launch(void* const* d_in, const int* in_sizes, int n_in,
                              void* d_out, int out_size, void* d_ws, size_t ws_size,
                              hipStream_t stream) {
  const u16* u = (const u16*)d_in[0];
  const u16* logA = (const u16*)d_in[1];
  const u16* Aim = (const u16*)d_in[2];
  const u16* Cre = (const u16*)d_in[3];
  const u16* Cim = (const u16*)d_in[4];
  const u16* logdt = (const u16*)d_in[5];
  const u16* dskip = (const u16*)d_in[6];
  const u16* w1 = (const u16*)d_in[8];
  const u16* b1 = (const u16*)d_in[9];
  const u16* w2 = (const u16*)d_in[10];
  const u16* b2 = (const u16*)d_in[11];
  u16* out = (u16*)d_out;
  char* ws = (char*)d_ws;
  u16* xn = (u16*)(ws);
  u16* xnT = (u16*)(ws + (2u << 20));
  u16* sT = (u16*)(ws + (4u << 20));
  u16* kexp = (u16*)(ws + (6u << 20));
  float* kinvp = (float*)(ws + (7u << 20) + (900u << 10));
  u16* hbuf = (u16*)(ws + (8u << 20));

  hipFuncSetAttribute((const void*)k0_ln_kgen, hipFuncAttributeMaxDynamicSharedMemorySize, K0_LDS);
  hipFuncSetAttribute((const void*)k1_gemm1_conv, hipFuncAttributeMaxDynamicSharedMemorySize, K1_LDS);
  hipFuncSetAttribute((const void*)k2_gemm2, hipFuncAttributeMaxDynamicSharedMemorySize, K2_LDS);
  k0_ln_kgen<<<LN_BLOCKS + DM, 256, K0_LDS, stream>>>(u, logA, Aim, Cre, Cim, logdt, xn, xnT, kexp, kinvp, w1);
  k1_gemm1_conv<<<G1_BLOCKS + 256, 256, K1_LDS, stream>>>(xn, xnT, kexp, kinvp, w1, b1, dskip, hbuf, sT, w2);
  k2_gemm2<<<G2_BLOCKS, 256, K2_LDS, stream>>>(hbuf, w2, b2, u, sT, out);
}
```
